```python
import torch

def _softmax(x, axis=-1):
    return torch.softmax(x, dim=axis)
def _gelu(x):
    return torch.nn.functional.gelu(x, approximate="tanh")
def _silu(x):
    return torch.nn.functional.silu(x)
def _sigmoid(x):
    return torch.sigmoid(x)
def _layernorm(x, axis=-1, eps=1e-5):
    m = x.mean(dim=axis, keepdim=True)
    v = x.var(dim=axis, keepdim=True, unbiased=False)
    return (x - m) / torch.sqrt(v + eps)
def _scatter_add(tgt, idx, src, axis=0):
    return tgt.clone().index_add_(axis, idx, src)
def _segment_sum(src, seg, n):
    out = torch.zeros((n,) + src.shape[1:], dtype=src.dtype)
    return out.index_add_(0, seg, src)
def _pad(x, pw):
    flat = tuple(v for pair in reversed(pw) for v in pair)
    return torch.nn.functional.pad(x, flat)

kernel_name = 'recurrent_scan__gqa_block_cached__geglu_ffn__decode'

def setup_inputs():

    _w = (lambda a: (lambda t: t.to(torch.bfloat16) if t.is_floating_point() else t)(torch.as_tensor(a)))
    def _A_setup():
        B, Sq, D = 2, 1, 512
        Hq, Hkv, Sk, Dh = 32, 32, 8192, 128
        assert Hq % Hkv == 0, (Hq, Hkv)
        return {
            "x": _w((lambda *s: torch.randn(*s, dtype=torch.bfloat16) * 0.05)(B, Sq, D)),
            "k_cache": _w((lambda *s: torch.randn(*s, dtype=torch.bfloat16) * 1.6)(B, Hkv, Sk, Dh)),
            "v_cache": _w((lambda *s: torch.randn(*s, dtype=torch.bfloat16) * 0.2)(B, Hkv, Sk, Dh)),
            "wq": _w((lambda *s: torch.randn(*s, dtype=torch.bfloat16) * 1.6)(D, Hq * Dh) * (D ** -0.5)),
            "wo": _w((lambda *s: torch.randn(*s, dtype=torch.bfloat16) * 0.2)(Hq * Dh, D) * ((Hq * Dh) ** -0.5)),
        }
    _A_in = _A_setup()

    def _B_setup():
        B, S, D, F = 2, 1, 512, 1376
        return {
            "x": _w((lambda *s: torch.randn(*s, dtype=torch.bfloat16) * 0.05)(B, S, D)),
            "w_gate": _w((lambda *s: torch.randn(*s, dtype=torch.bfloat16) * 0.1)(D, F)),
            "w_up": _w((lambda *s: torch.randn(*s, dtype=torch.bfloat16) * 0.1)(D, F)),
            "w_down": _w((lambda *s: torch.randn(*s, dtype=torch.bfloat16) * 0.1)(F, D)),
        }
    _B_in = _B_setup()

    return {
        "A_x": _A_in["x"],
        "A_k_cache": _A_in["k_cache"],
        "A_v_cache": _A_in["v_cache"],
        "A_wq": _A_in["wq"],
        "A_wo": _A_in["wo"],
        "B_x": _B_in["x"],
        "B_w_gate": _B_in["w_gate"],
        "B_w_up": _B_in["w_up"],
        "B_w_down": _B_in["w_down"],
    }

def reference(A_x, A_k_cache, A_v_cache, A_wq, A_wo, B_x, B_w_gate, B_w_up, B_w_down):
    def _A_ref(x, k_cache, v_cache, wq, wo):
        B, Sq, D = x.shape
        Hq, Dh = 32, 128
        Hkv = k_cache.shape[1]
        rep = Hq // Hkv
        q = (lambda x, *d: x.permute(*d))((x @ wq).reshape(B, Sq, Hq, Dh), 0, 2, 1, 3)
        k = (lambda x, n, axis: torch.repeat_interleave(x, n, dim=axis))(k_cache, rep, axis=1)
        v = (lambda x, n, axis: torch.repeat_interleave(x, n, dim=axis))(v_cache, rep, axis=1)
        scores = torch.einsum("bhqd,bhkd->bhqk", q, k) * (Dh ** -0.5)
        p = _softmax(scores, axis=-1)
        o = torch.einsum("bhqk,bhkd->bhqd", p, v)
        return (lambda x, *d: x.permute(*d))(o, 0, 2, 1, 3).reshape(B, Sq, Hq * Dh) @ wo

    def _B_ref(x, w_gate, w_up, w_down):
        return (_gelu(x @ w_gate) * (x @ w_up)) @ w_down

    x = A_x
    xn = _layernorm(x, axis=-1)
    s = _A_ref(xn, A_k_cache, A_v_cache, A_wq, A_wo)
    g = _silu(_B_ref(xn, B_w_gate, B_w_up, B_w_down))
    return x + s * g
```

```cpp
#include <hip/hip_runtime.h>
#include <hip/hip_bf16.h>
#include <cstdint>

typedef unsigned int u32;
using u32x4 = __attribute__((ext_vector_type(4))) unsigned;
using f32x4 = __attribute__((ext_vector_type(4))) float;
using f32x2 = __attribute__((ext_vector_type(2))) float;
using bf16x2 = __attribute__((ext_vector_type(2))) __bf16;

constexpr int NB = 2, DM = 512, NH = 32, SK = 8192, DH = 128, FF = 1376;
constexpr int NSPLIT = 4, KEYS_WG = SK / NSPLIT;
constexpr int NWAVE = 16, NTHR = NWAVE * 64;
constexpr int NTILE = KEYS_WG / (NWAVE * 16);
constexpr int NUNIT = FF / 8;
constexpr size_t WS_Y = 0;
constexpr size_t WS_ML = WS_Y + 256 * 512;
constexpr size_t WS_H = WS_ML + 256 * 2;

__device__ __forceinline__ float dot2(unsigned k, unsigned q, float acc) {
  return __builtin_amdgcn_fdot2_f32_bf16(__builtin_bit_cast(bf16x2, k), __builtin_bit_cast(bf16x2, q), acc, false);
}
template <int CTRL> __device__ __forceinline__ float dpp(float x) {
  return __builtin_bit_cast(float, __builtin_amdgcn_mov_dpp(__builtin_bit_cast(int, x), CTRL, 0xf, 0xf, true));
}
constexpr int XOR1 = 0xB1, XOR2 = 0x4E, HMIR = 0x141, ROR8 = 0x128;
__device__ __forceinline__ float row16_sum(float x) {
  x += dpp<XOR1>(x); x += dpp<XOR2>(x); x += dpp<HMIR>(x); x += dpp<ROR8>(x); return x;
}
__device__ __forceinline__ float xrow_sum(float x) {
  auto s = __builtin_amdgcn_permlane16_swap(__float_as_uint(x), __float_as_uint(x), false, false);
  x = __uint_as_float(s[0]) + __uint_as_float(s[1]);
  auto t = __builtin_amdgcn_permlane32_swap(__float_as_uint(x), __float_as_uint(x), false, false);
  return __uint_as_float(t[0]) + __uint_as_float(t[1]);
}
__device__ __forceinline__ float xrow_max(float x) {
  auto s = __builtin_amdgcn_permlane16_swap(__float_as_uint(x), __float_as_uint(x), false, false);
  x = fmaxf(__uint_as_float(s[0]), __uint_as_float(s[1]));
  auto t = __builtin_amdgcn_permlane32_swap(__float_as_uint(x), __float_as_uint(x), false, false);
  return fmaxf(__uint_as_float(t[0]), __uint_as_float(t[1]));
}
__device__ __forceinline__ float wave_sum(float x) { return xrow_sum(row16_sum(x)); }
__device__ __forceinline__ float bf_lo(unsigned u) { return __uint_as_float(u << 16); }
__device__ __forceinline__ float bf_hi(unsigned u) { return __uint_as_float(u & 0xffff0000u); }
__device__ __forceinline__ float rbf(float x) { return (float)(__bf16)x; }
__device__ __forceinline__ unsigned pk_bf(float lo, float hi) { bf16x2 v = {(__bf16)lo, (__bf16)hi}; return __builtin_bit_cast(unsigned, v); }
__device__ __forceinline__ u32x4 ld_nt(const void* p) { return __builtin_nontemporal_load(reinterpret_cast<const u32x4*>(p)); }
__device__ __forceinline__ u32x4 ld(const void* p) { return *reinterpret_cast<const u32x4*>(p); }

__device__ __forceinline__ void layernorm8(u32x4 xr, float (&xn)[8]) {
  float v[8];
#pragma unroll
  for (int k = 0; k < 4; ++k) { v[2 * k] = bf_lo(xr[k]); v[2 * k + 1] = bf_hi(xr[k]); }
  float s = 0.f;
#pragma unroll
  for (int k = 0; k < 8; ++k) s += v[k];
  const float mean = wave_sum(s) * (1.f / DM);
  float q = 0.f;
#pragma unroll
  for (int k = 0; k < 8; ++k) { const float d = v[k] - mean; q = fmaf(d, d, q); }
  const float var = wave_sum(q) * (1.f / DM);
  const float mb = rbf(mean), den = rbf(sqrtf(rbf(rbf(var) + 1e-5f)));
#pragma unroll
  for (int k = 0; k < 8; ++k) xn[k] = rbf(rbf(v[k] - mb) / den);
}

__global__ void __launch_bounds__(NTHR) attn_kernel(const __hip_bfloat16* __restrict__ x, const __hip_bfloat16* __restrict__ Kc,
                                                    const __hip_bfloat16* __restrict__ Vc, const __hip_bfloat16* __restrict__ wq,
                                                    const __hip_bfloat16* __restrict__ wo, const __hip_bfloat16* __restrict__ wg,
                                                    const __hip_bfloat16* __restrict__ wu, float* __restrict__ ws) {
  __shared__ __attribute__((aligned(16))) float lds[16 * 512 + 16 * 132 + 64];
  float* xs = lds;
  float* qp = lds + 1024;
  float* fr = lds + 1024 + 2048;
  float* yr = lds;
  float* mrg = lds + 16 * 512;

  const int tid = threadIdx.x, lane = tid & 63, wave = __builtin_amdgcn_readfirstlane(tid >> 6), r = lane >> 4, c = lane & 15;
  const int bid = blockIdx.x, xcd = bid & 7, j = bid >> 3;
  const int h = xcd * 4 + (j & 3), bs = j >> 2, b = bs >> 2, split = bs & 3;
  const int unit = (j < 22) ? xcd * 22 + j : NUNIT;
  const bool has_ffn = unit < NUNIT;

  u32x4 x0, x1;
  if (wave == 0) { x0 = ld(x + 8 * lane); x1 = ld(x + DM + 8 * lane); }
  u32x4 W[8];
  {
    const __hip_bfloat16* p = wq + (size_t)(32 * wave + r) * (NH * DH) + h * DH + 8 * c;
#pragma unroll
    for (int i = 0; i < 8; ++i) W[i] = ld(p + (size_t)(4 * i) * (NH * DH));
  }
  u32x4 fw = {0u, 0u, 0u, 0u};
  const int fk = 64 * (wave & 7) + lane;
  if (has_ffn) fw = ld(((wave < 8) ? wg : wu) + (size_t)fk * FF + 8 * unit);

  const size_t kvbase = ((size_t)(b * NH + h) * SK + (size_t)split * KEYS_WG + wave * 16 + r) * DH + 8 * c;
  const __hip_bfloat16* kp = Kc + kvbase;
  const __hip_bfloat16* vp = Vc + kvbase;
  u32x4 A[8], B[8];
#define ISSUE(BUF, T) do { _Pragma("unroll") for (int t_ = 0; t_ < 4; ++t_) BUF[t_] = ld_nt(kp + (size_t)((T) * 256 + 4 * t_) * DH); \
                           _Pragma("unroll") for (int t_ = 0; t_ < 4; ++t_) BUF[4 + t_] = ld_nt(vp + (size_t)((T) * 256 + 4 * t_) * DH); } while (0)
  ISSUE(A, 0);

  if (wave == 0) {
    float n0[8], n1[8];
    layernorm8(x0, n0); layernorm8(x1, n1);
    *reinterpret_cast<f32x4*>(xs + 8 * lane) = f32x4{n0[0], n0[1], n0[2], n0[3]};
    *reinterpret_cast<f32x4*>(xs + 8 * lane + 4) = f32x4{n0[4], n0[5], n0[6], n0[7]};
    *reinterpret_cast<f32x4*>(xs + DM + 8 * lane) = f32x4{n1[0], n1[1], n1[2], n1[3]};
    *reinterpret_cast<f32x4*>(xs + DM + 8 * lane + 4) = f32x4{n1[4], n1[5], n1[6], n1[7]};
  }
  __syncthreads();

  {
    float a[8];
#pragma unroll
    for (int k = 0; k < 8; ++k) a[k] = 0.f;
#pragma unroll
    for (int i = 0; i < 8; ++i) {
      const float xv = xs[b * DM + 32 * wave + 4 * i + r];
#pragma unroll
      for (int k = 0; k < 4; ++k) { a[2 * k] = fmaf(xv, bf_lo(W[i][k]), a[2 * k]); a[2 * k + 1] = fmaf(xv, bf_hi(W[i][k]), a[2 * k + 1]); }
    }
#pragma unroll
    for (int k = 0; k < 8; ++k) a[k] = xrow_sum(a[k]);
    if (r == 0) {
      *reinterpret_cast<f32x4*>(qp + wave * DH + 8 * c) = f32x4{a[0], a[1], a[2], a[3]};
      *reinterpret_cast<f32x4*>(qp + wave * DH + 8 * c + 4) = f32x4{a[4], a[5], a[6], a[7]};
    }
  }
  ISSUE(B, 1);
  if (has_ffn) {
    const float xa = xs[fk], xb = xs[DM + fk];
    float v[16];
#pragma unroll
    for (int k = 0; k < 4; ++k) {
      v[2 * k] = xa * bf_lo(fw[k]); v[2 * k + 1] = xa * bf_hi(fw[k]);
      v[8 + 2 * k] = xb * bf_lo(fw[k]); v[8 + 2 * k + 1] = xb * bf_hi(fw[k]);
    }
#pragma unroll
    for (int k = 0; k < 16; ++k) v[k] = wave_sum(v[k]);
    if (lane == 0) {
#pragma unroll
      for (int k = 0; k < 16; k += 4) *reinterpret_cast<f32x4*>(fr + wave * 16 + k) = f32x4{v[k], v[k + 1], v[k + 2], v[k + 3]};
    }
  }
  __syncthreads();
  u32x4 q;
  {
    f32x4 s0 = {0.f, 0.f, 0.f, 0.f}, s1 = {0.f, 0.f, 0.f, 0.f};
#pragma unroll
    for (int w = 0; w < 4; ++w) {
      s0 += *reinterpret_cast<const f32x4*>(qp + (4 * r + w) * DH + 8 * c);
      s1 += *reinterpret_cast<const f32x4*>(qp + (4 * r + w) * DH + 8 * c + 4);
    }
    float a[8] = {s0[0], s0[1], s0[2], s0[3], s1[0], s1[1], s1[2], s1[3]};
#pragma unroll
    for (int k = 0; k < 8; ++k) a[k] = xrow_sum(a[k]);
#pragma unroll
    for (int k = 0; k < 4; ++k) q[k] = pk_bf(a[2 * k], a[2 * k + 1]);
  }
  if (has_ffn && tid < 16) {
    float g = 0.f, u = 0.f;
#pragma unroll
    for (int w = 0; w < 8; ++w) { g += fr[w * 16 + tid]; u += fr[(8 + w) * 16 + tid]; }
    g = rbf(g); u = rbf(u);
    const float ge = rbf(0.5f * g * (1.f + tanhf(0.7978845608028654f * (g + 0.044715f * g * g * g))));
    ws[WS_H + (tid >> 3) * FF + 8 * unit + (tid & 7)] = rbf(ge * u);
  }

  constexpr float SCALE = 0.08838834764831845f, LOG2E = 1.4426950408889634f;
  float m = -INFINITY, l = 0.f, acc[8];
#pragma unroll
  for (int k = 0; k < 8; ++k) acc[k] = 0.f;
#define PROCESS(BUF) do { float s_[4]; \
    _Pragma("unroll") for (int t_ = 0; t_ < 4; ++t_) { float d_ = dot2(BUF[t_][0], q[0], 0.f); d_ = dot2(BUF[t_][1], q[1], d_); d_ = dot2(BUF[t_][2], q[2], d_); d_ = dot2(BUF[t_][3], q[3], d_); \
      d_ = row16_sum(d_); s_[t_] = rbf(rbf(d_) * SCALE) * LOG2E; } \
    const float mn_ = fmaxf(fmaxf(fmaxf(s_[0], s_[1]), fmaxf(s_[2], s_[3])), m); \
    const float al_ = __builtin_amdgcn_exp2f(m - mn_); m = mn_; l *= al_; \
    _Pragma("unroll") for (int k_ = 0; k_ < 8; ++k_) acc[k_] *= al_; \
    _Pragma("unroll") for (int t_ = 0; t_ < 4; ++t_) { const float p_ = __builtin_amdgcn_exp2f(s_[t_] - mn_); l += p_; \
      _Pragma("unroll") for (int k_ = 0; k_ < 4; ++k_) { acc[2 * k_] = fmaf(p_, bf_lo(BUF[4 + t_][k_]), acc[2 * k_]); acc[2 * k_ + 1] = fmaf(p_, bf_hi(BUF[4 + t_][k_]), acc[2 * k_ + 1]); } } } while (0)

#pragma unroll
  for (int i = 0; i < NTILE; i += 2) {
    PROCESS(A);
    if (i + 2 < NTILE) ISSUE(A, i + 2);
    else {
      const __hip_bfloat16* p = wo + (size_t)(h * DH + 8 * wave) * DM + 8 * lane;
#pragma unroll
      for (int t = 0; t < 8; ++t) A[t] = ld(p + (size_t)t * DM);
    }
    PROCESS(B);
    if (i + 3 < NTILE) ISSUE(B, i + 3);
  }

  {
    const float mw = xrow_max(m), sc = __builtin_amdgcn_exp2f(m - mw);
    l = xrow_sum(l * sc);
#pragma unroll
    for (int k = 0; k < 8; ++k) acc[k] = xrow_sum(acc[k] * sc);
    float* rec = mrg + wave * 132;
    if (r == 0) {
      *reinterpret_cast<f32x4*>(rec + 4 + 8 * c) = f32x4{acc[0], acc[1], acc[2], acc[3]};
      *reinterpret_cast<f32x4*>(rec + 4 + 8 * c + 4) = f32x4{acc[4], acc[5], acc[6], acc[7]};
      if (c == 0) { rec[0] = mw; rec[1] = l; }
    }
  }
  __syncthreads();
  {
    const float mc = mrg[c * 132], lc = mrg[c * 132 + 1];
    float M = mc; M = fmaxf(M, dpp<XOR1>(M)); M = fmaxf(M, dpp<XOR2>(M)); M = fmaxf(M, dpp<HMIR>(M)); M = fmaxf(M, dpp<ROR8>(M));
    const float wgt = __builtin_amdgcn_exp2f(mc - M);
    const f32x2 av = *reinterpret_cast<const f32x2*>(mrg + c * 132 + 4 + 8 * wave + 2 * r);
    const float o0 = row16_sum(av[0] * wgt), o1 = row16_sum(av[1] * wgt);
    const float L = row16_sum(lc * wgt);
    float y[8];
#pragma unroll
    for (int k = 0; k < 8; ++k) y[k] = 0.f;
#pragma unroll
    for (int t = 0; t < 8; ++t) {
      const float ov = __builtin_bit_cast(float, __builtin_amdgcn_readlane(__builtin_bit_cast(int, (t & 1) ? o1 : o0), 16 * (t >> 1)));
#pragma unroll
      for (int k = 0; k < 4; ++k) { y[2 * k] = fmaf(ov, bf_lo(A[t][k]), y[2 * k]); y[2 * k + 1] = fmaf(ov, bf_hi(A[t][k]), y[2 * k + 1]); }
    }
    *reinterpret_cast<f32x4*>(yr + wave * DM + 8 * lane) = f32x4{y[0], y[1], y[2], y[3]};
    *reinterpret_cast<f32x4*>(yr + wave * DM + 8 * lane + 4) = f32x4{y[4], y[5], y[6], y[7]};
    const int e = (b * NH + h) * NSPLIT + split;
    if (tid == 0) *reinterpret_cast<f32x2*>(ws + WS_ML + 2 * e) = f32x2{M, L};
    __syncthreads();
    if (tid < DM) {
      float s = 0.f;
#pragma unroll
      for (int w = 0; w < NWAVE; ++w) s += yr[w * DM + tid];
      ws[WS_Y + (size_t)e * DM + tid] = s;
    }
  }
}

__global__ void __launch_bounds__(256) final_kernel(const __hip_bfloat16* __restrict__ x, const __hip_bfloat16* __restrict__ wd,
                                                    const float* __restrict__ ws, __hip_bfloat16* __restrict__ out) {
  __shared__ __attribute__((aligned(16))) float red[4][24];
  const int tid = threadIdx.x, lane = tid & 63, wave = tid >> 6, j = blockIdx.x;
  const f32x4 y0 = *reinterpret_cast<const f32x4*>(ws + WS_Y + (size_t)tid * DM + 8 * j);
  const f32x4 y1 = *reinterpret_cast<const f32x4*>(ws + WS_Y + (size_t)tid * DM + 8 * j + 4);
  const f32x4 ml0 = *reinterpret_cast<const f32x4*>(ws + WS_ML + 8 * (tid >> 2));
  const f32x4 ml1 = *reinterpret_cast<const f32x4*>(ws + WS_ML + 8 * (tid >> 2) + 4);
  u32x4 wv[6]; float h0[6], h1[6];
#pragma unroll
  for (int i = 0; i < 6; ++i) {
    const int f = tid + 256 * i;
    if (f < FF) { wv[i] = ld(wd + (size_t)f * DM + 8 * j); h0[i] = ws[WS_H + f]; h1[i] = ws[WS_H + FF + f]; }
    else { wv[i] = u32x4{0u, 0u, 0u, 0u}; h0[i] = 0.f; h1[i] = 0.f; }
  }
  const float M = fmaxf(fmaxf(ml0[0], ml0[2]), fmaxf(ml1[0], ml1[2]));
  const float e0 = __builtin_amdgcn_exp2f(ml0[0] - M), e1 = __builtin_amdgcn_exp2f(ml0[2] - M), e2 = __builtin_amdgcn_exp2f(ml1[0] - M), e3 = __builtin_amdgcn_exp2f(ml1[2] - M);
  const float L = e0 * ml0[1] + e1 * ml0[3] + e2 * ml1[1] + e3 * ml1[3];
  const int s = tid & 3;
  const float wgt = (s == 0 ? e0 : s == 1 ? e1 : s == 2 ? e2 : e3) / L;
  float v[24];
#pragma unroll
  for (int k = 0; k < 4; ++k) { v[k] = y0[k] * wgt; v[4 + k] = y1[k] * wgt; }
#pragma unroll
  for (int k = 8; k < 24; ++k) v[k] = 0.f;
#pragma unroll
  for (int i = 0; i < 6; ++i)
#pragma unroll
    for (int k = 0; k < 4; ++k) {
      const float lo = bf_lo(wv[i][k]), hi = bf_hi(wv[i][k]);
      v[8 + 2 * k] = fmaf(h0[i], lo, v[8 + 2 * k]); v[8 + 2 * k + 1] = fmaf(h0[i], hi, v[8 + 2 * k + 1]);
      v[16 + 2 * k] = fmaf(h1[i], lo, v[16 + 2 * k]); v[16 + 2 * k + 1] = fmaf(h1[i], hi, v[16 + 2 * k + 1]);
    }
#pragma unroll
  for (int k = 0; k < 24; ++k) v[k] = wave_sum(v[k]);
  if (lane == 0) {
#pragma unroll
    for (int k = 0; k < 24; k += 4) *reinterpret_cast<f32x4*>(&red[wave][k]) = f32x4{v[k], v[k + 1], v[k + 2], v[k + 3]};
  }
  __syncthreads();
  if (tid < 16) {
    const int b = tid >> 3, k = tid & 7, n = 8 * j + k;
    const float sv = rbf(red[2 * b][k] + red[2 * b + 1][k]);
    const float f = rbf(red[0][8 + 8 * b + k] + red[1][8 + 8 * b + k] + red[2][8 + 8 * b + k] + red[3][8 + 8 * b + k]);
    const float g = rbf(f / (1.f + __expf(-f)));
    const float xv = __bfloat162float(x[b * DM + n]);
    out[b * DM + n] = __float2bfloat16(xv + rbf(sv * g));
  }
}

extern "C" void kernel_launch(void* const* d_in, const int* in_sizes, int n_in, void* d_out, int out_size, void* d_ws, size_t ws_size,
                              hipStream_t stream) {
  const __hip_bfloat16* x = (const __hip_bfloat16*)d_in[0];
  const __hip_bfloat16* K = (const __hip_bfloat16*)d_in[1];
  const __hip_bfloat16* V = (const __hip_bfloat16*)d_in[2];
  const __hip_bfloat16* wq = (const __hip_bfloat16*)d_in[3];
  const __hip_bfloat16* wo = (const __hip_bfloat16*)d_in[4];
  const __hip_bfloat16* wg = (const __hip_bfloat16*)d_in[6];
  const __hip_bfloat16* wu = (const __hip_bfloat16*)d_in[7];
  const __hip_bfloat16* wd = (const __hip_bfloat16*)d_in[8];
  float* ws = (float*)d_ws;
  attn_kernel<<<256, NTHR, 0, stream>>>(x, K, V, wq, wo, wg, wu, ws);
  final_kernel<<<64, 256, 0, stream>>>(x, wd, ws, (__hip_bfloat16*)d_out);
}
```

```cpp
#include <hip/hip_runtime.h>
#include <hip/hip_bf16.h>
#include <cstdint>

typedef unsigned int u32;
using u32x4 = __attribute__((ext_vector_type(4))) unsigned;
using f32x4 = __attribute__((ext_vector_type(4))) float;
using f32x2 = __attribute__((ext_vector_type(2))) float;
using bf16x2 = __attribute__((ext_vector_type(2))) __bf16;

constexpr int NB = 2, DM = 512, NH = 32, SK = 8192, DH = 128, FF = 1376;
constexpr int NSPLIT = 4, KEYS_WG = SK / NSPLIT;
constexpr int NW = 8, NTHR = NW * 64, NS = 2;
constexpr int TKEYS = 4 * NS;
constexpr int NT = SK / TKEYS;
#define CFG_NPRE 4
#define CFG_DG 6
#define CFG_DD 4
constexpr int NPRE = CFG_NPRE;
constexpr int T_STAT = 4 * NPRE;
constexpr int NSTATIC = T_STAT + 18 * NW;
constexpr int NG = FF / 32, ND = 21, DROWS = 66;
constexpr size_t WS_EPOCH = 0;
constexpr size_t WS_H = 256;
constexpr size_t WS_ML = WS_H + 1376 * 16;
constexpr size_t WS_FD = WS_ML + 256 * 16;
constexpr size_t WS_Y = WS_FD + ND * 2 * DM * 8;
#define STAMP_A(k)
#define SB() __builtin_amdgcn_sched_barrier(0)

__device__ __forceinline__ float dot2(unsigned k, unsigned q, float acc) {
  return __builtin_amdgcn_fdot2_f32_bf16(__builtin_bit_cast(bf16x2, k), __builtin_bit_cast(bf16x2, q), acc, false);
}
template <int CTRL> __device__ __forceinline__ float dpp(float x) {
  return __builtin_bit_cast(float, __builtin_amdgcn_mov_dpp(__builtin_bit_cast(int, x), CTRL, 0xf, 0xf, true));
}
constexpr int XOR1 = 0xB1, XOR2 = 0x4E, HMIR = 0x141, ROR8 = 0x128, ROR4 = 0x124;
__device__ __forceinline__ float row16_sum(float x) {
  x += dpp<XOR1>(x); x += dpp<XOR2>(x); x += dpp<HMIR>(x); x += dpp<ROR8>(x); return x;
}
__device__ __forceinline__ float swap16_add(float a, float b) {
  auto s = __builtin_amdgcn_permlane16_swap(__float_as_uint(a), __float_as_uint(b), false, false);
  return __uint_as_float(s[0]) + __uint_as_float(s[1]);
}
__device__ __forceinline__ float swap32_add(float a, float b) {
  auto s = __builtin_amdgcn_permlane32_swap(__float_as_uint(a), __float_as_uint(b), false, false);
  return __uint_as_float(s[0]) + __uint_as_float(s[1]);
}
__device__ __forceinline__ float xrow_sum(float x) { x = swap16_add(x, x); return swap32_add(x, x); }
__device__ __forceinline__ float xrow_max(float x) {
  auto s = __builtin_amdgcn_permlane16_swap(__float_as_uint(x), __float_as_uint(x), false, false);
  x = fmaxf(__uint_as_float(s[0]), __uint_as_float(s[1]));
  auto t = __builtin_amdgcn_permlane32_swap(__float_as_uint(x), __float_as_uint(x), false, false);
  return fmaxf(__uint_as_float(t[0]), __uint_as_float(t[1]));
}
__device__ __forceinline__ float wave_sum(float x) { return xrow_sum(row16_sum(x)); }
__device__ __forceinline__ float treduce32(float (&v)[32], int lane) {
#pragma unroll
  for (int i = 0; i < 16; ++i) v[i] = swap32_add(v[i], v[i + 16]);
#pragma unroll
  for (int i = 0; i < 8; ++i) v[i] = swap16_add(v[i], v[i + 8]);
  const bool b8 = lane & 8, b4 = lane & 4, b2 = lane & 2;
#pragma unroll
  for (int i = 0; i < 4; ++i) { const float t1 = v[i] + dpp<ROR8>(v[i]), t2 = v[i + 4] + dpp<ROR8>(v[i + 4]); v[i] = b8 ? t2 : t1; }
#pragma unroll
  for (int i = 0; i < 2; ++i) { const float t1 = v[i] + dpp<HMIR>(v[i]), t2 = v[i + 2] + dpp<HMIR>(v[i + 2]); v[i] = b4 ? t2 : t1; }
  { const float t1 = v[0] + dpp<XOR2>(v[0]), t2 = v[1] + dpp<XOR2>(v[1]); v[0] = b2 ? t2 : t1; }
  return v[0] + dpp<XOR1>(v[0]);
}
__device__ __forceinline__ float treduce16(float (&v)[16], int lane) {
#pragma unroll
  for (int i = 0; i < 8; ++i) v[i] = swap32_add(v[i], v[i + 8]);
#pragma unroll
  for (int i = 0; i < 4; ++i) v[i] = swap16_add(v[i], v[i + 4]);
  const bool b8 = lane & 8, b4 = lane & 4;
#pragma unroll
  for (int i = 0; i < 2; ++i) { const float t1 = v[i] + dpp<ROR8>(v[i]), t2 = v[i + 2] + dpp<ROR8>(v[i + 2]); v[i] = b8 ? t2 : t1; }
  { const float t1 = v[0] + dpp<HMIR>(v[0]), t2 = v[1] + dpp<HMIR>(v[1]); v[0] = b4 ? t2 : t1; }
  v[0] += dpp<XOR2>(v[0]);
  return v[0] + dpp<XOR1>(v[0]);
}
__device__ __forceinline__ float bf_lo(unsigned u) { return __uint_as_float(u << 16); }
__device__ __forceinline__ float bf_hi(unsigned u) { return __uint_as_float(u & 0xffff0000u); }
__device__ __forceinline__ float rbf(float x) { return (float)(__bf16)x; }
__device__ __forceinline__ unsigned pk_bf(float lo, float hi) { bf16x2 v = {(__bf16)lo, (__bf16)hi}; return __builtin_bit_cast(unsigned, v); }
__device__ __forceinline__ u32x4 ld_nt(const void* p) { return __builtin_nontemporal_load(reinterpret_cast<const u32x4*>(p)); }
__device__ __forceinline__ u32x4 ld(const void* p) { return *reinterpret_cast<const u32x4*>(p); }
typedef __attribute__((address_space(8))) void* rsrc_t_;
__device__ __forceinline__ void st_sc1(__amdgpu_buffer_rsrc_t rs, unsigned off, u32x4 v) { __builtin_amdgcn_raw_buffer_store_b128(v, rs, off, 0, 16); }
__device__ __forceinline__ u32x4 ld_sc1(__amdgpu_buffer_rsrc_t rs, unsigned off) { return __builtin_amdgcn_raw_buffer_load_b128(rs, off, 0, 16); }

__device__ __forceinline__ unsigned long long atomic_add_async(unsigned long long* p, unsigned long long v) {
  unsigned long long r, sv;
  asm volatile("s_mov_b64 %1, exec\n\ts_mov_b64 exec, 1\n\tglobal_atomic_add_x2 %0, %2, %3, off sc0\n\ts_mov_b64 exec, %1"
               : "=&v"(r), "=&s"(sv) : "v"(p), "v"(v) : "memory");
  return r;
}
__device__ __forceinline__ void layernorm8(u32x4 xr, float (&xn)[8]) {
  float v[8];
#pragma unroll
  for (int k = 0; k < 4; ++k) { v[2 * k] = bf_lo(xr[k]); v[2 * k + 1] = bf_hi(xr[k]); }
  float s = 0.f;
#pragma unroll
  for (int k = 0; k < 8; ++k) s += v[k];
  const float mean = wave_sum(s) * (1.f / DM);
  float q = 0.f;
#pragma unroll
  for (int k = 0; k < 8; ++k) { const float d = v[k] - mean; q = fmaf(d, d, q); }
  const float var = wave_sum(q) * (1.f / DM);
  const float mb = rbf(mean), den = rbf(__builtin_amdgcn_sqrtf(rbf(rbf(var) + 1e-5f))), rden = __builtin_amdgcn_rcpf(den);
#pragma unroll
  for (int k = 0; k < 8; ++k) xn[k] = rbf(rbf(v[k] - mb) * rden);
}

constexpr int L_YR = 0;
constexpr int L_XP = 0;
constexpr int L_QP = L_XP + 4 * 256;
constexpr int L_MRG = NW * 512;
constexpr int L_XS = L_MRG + NW * 132;
constexpr int L_FIN = L_XS + 1024;
constexpr int L_CNT = L_FIN + 64 + 16;
constexpr int L_FR = L_CNT + 4;
constexpr int L_END = L_FR + NW * 1024;

struct Args {
  const __hip_bfloat16 *x, *Kc, *Vc, *wq, *wo, *wg, *wu, *wd;
  __hip_bfloat16* out;
  unsigned char* ws;
};

template <int ROLE>
__device__ __forceinline__ void body(const Args& a, float* lds, const int rid  ) {
  unsigned char* ws = a.ws;
  const int tid = threadIdx.x, lane = tid & 63, wave = __builtin_amdgcn_readfirstlane(tid >> 6), r = lane >> 4, c = lane & 15;
  const int bid = blockIdx.x, xcd = bid & 7, j = bid >> 3;
  const int h = xcd * 4 + (j & 3), b = j >> 4, split = (j >> 2) & 3;
  const __amdgpu_buffer_rsrc_t wsr = __builtin_amdgcn_make_buffer_rsrc(ws, 0, 4u << 20, 0x00020000);
  STAMP_A(0);

  const size_t kvbase = ((size_t)(b * NH + h) * SK + r) * DH + 8 * c;
  const int dd = rid < NG ? CFG_DG : CFG_DD;
  const int ts = split == 0 ? 0 : 256 - 3 * dd + (split - 1) * (256 + dd);
  const unsigned tend = split == 0 ? 256 - 3 * dd : ts + 256 + dd;
  const __hip_bfloat16* kp = a.Kc + kvbase;
  const __hip_bfloat16* vp = a.Vc + kvbase;
#define ISSUE(BUF, T) do { const size_t o_ = (size_t)(T) * (TKEYS * DH); \
    _Pragma("unroll") for (int t_ = 0; t_ < NS; ++t_) (BUF)[t_] = ld_nt(kp + o_ + 4 * t_ * DH); \
    _Pragma("unroll") for (int t_ = 0; t_ < NS; ++t_) (BUF)[NS + t_] = ld_nt(vp + o_ + 4 * t_ * DH); } while (0)

  u32x4 E[32];
  unsigned epoch = 0;
  if (wave < 4) {
    u32x4 xr;
    if (ROLE == 1) xr = ld(a.x + (wave & 1) * DM + 8 * lane);
    epoch = __hip_atomic_load(reinterpret_cast<const unsigned*>(ws + WS_EPOCH), __ATOMIC_RELAXED, __HIP_MEMORY_SCOPE_AGENT);
    if (tid == 0) { reinterpret_cast<unsigned*>(lds)[L_CNT] = ts + NSTATIC; reinterpret_cast<unsigned*>(lds)[L_CNT + 1] = 0; }
    SB();
    __builtin_amdgcn_s_barrier();
    SB();
#pragma unroll
    for (int i = 0; i < NPRE; ++i) ISSUE(E + 4 * i, ts + 4 * i + wave);
    SB();
    if (ROLE == 1) {
      float n0[8];
      layernorm8(xr, n0);
      if (wave < 2) {
        *reinterpret_cast<f32x4*>(lds + L_XS + wave * DM + 8 * lane) = f32x4{n0[0], n0[1], n0[2], n0[3]};
        *reinterpret_cast<f32x4*>(lds + L_XS + wave * DM + 8 * lane + 4) = f32x4{n0[4], n0[5], n0[6], n0[7]};
      }
    }
    STAMP_A(1); STAMP_A(2);
  } else {
    const int qw = wave - 4;
    const u32x4 x0 = ld(a.x + b * DM + 8 * lane);
    SB();
    {
      const __hip_bfloat16* p = a.wq + (size_t)(128 * qw + r) * (NH * DH) + h * DH + 8 * c;
#pragma unroll
      for (int i = 0; i < 32; ++i) E[i] = ld_nt(p + (size_t)(4 * i) * (NH * DH));
    }
    epoch = __hip_atomic_load(reinterpret_cast<const unsigned*>(ws + WS_EPOCH), __ATOMIC_RELAXED, __HIP_MEMORY_SCOPE_AGENT);
    SB();
    __builtin_amdgcn_s_barrier();
    SB();
    STAMP_A(1);
    {
      float n0[8];
      layernorm8(x0, n0);
      u32x4 pk;
#pragma unroll
      for (int k = 0; k < 4; ++k) pk[k] = pk_bf(n0[k], n0[k + 4]);
      *reinterpret_cast<u32x4*>(lds + L_XP + qw * 256 + 4 * lane) = pk;
    }
    float qa[8];
#pragma unroll
    for (int k = 0; k < 8; ++k) qa[k] = 0.f;
    const unsigned* xp = reinterpret_cast<const unsigned*>(lds + L_XP + qw * 256 + 64 * qw + r);
#pragma unroll
    for (int p = 0; p < 16; ++p) {
      const unsigned xv = xp[4 * p];
#pragma unroll
      for (int k = 0; k < 4; ++k) {
        const unsigned lo = __builtin_amdgcn_perm(E[2 * p + 1][k], E[2 * p][k], 0x05040100u);
        const unsigned hi = __builtin_amdgcn_perm(E[2 * p + 1][k], E[2 * p][k], 0x07060302u);
        qa[2 * k] = dot2(lo, xv, qa[2 * k]); qa[2 * k + 1] = dot2(hi, xv, qa[2 * k + 1]);
      }
    }
#pragma unroll
    for (int k = 0; k < 8; ++k) qa[k] = xrow_sum(qa[k]);
    if (r == 0) {
      *reinterpret_cast<f32x4*>(lds + L_QP + qw * DH + 8 * c) = f32x4{qa[0], qa[1], qa[2], qa[3]};
      *reinterpret_cast<f32x4*>(lds + L_QP + qw * DH + 8 * c + 4) = f32x4{qa[4], qa[5], qa[6], qa[7]};
    }
    STAMP_A(2);
  }
  __syncthreads();
  STAMP_A(3);
  u32x4 A[2 * NS], B[2 * NS];
  ISSUE(A, ts + T_STAT + wave);
  ISSUE(B, ts + T_STAT + NW + wave);
  SB();
  u32x4 q;
  {
    const f32x4 s0 = *reinterpret_cast<const f32x4*>(lds + L_QP + r * DH + 8 * c);
    const f32x4 s1 = *reinterpret_cast<const f32x4*>(lds + L_QP + r * DH + 8 * c + 4);
    float qa[8] = {s0[0], s0[1], s0[2], s0[3], s1[0], s1[1], s1[2], s1[3]};
#pragma unroll
    for (int k = 0; k < 8; ++k) qa[k] = xrow_sum(qa[k]);
#pragma unroll
    for (int k = 0; k < 4; ++k) q[k] = pk_bf(qa[2 * k], qa[2 * k + 1]);
  }
  const unsigned T = __builtin_amdgcn_readfirstlane(epoch) + 1u;
  STAMP_A(4);

  constexpr float SCALE = 0.08838834764831845f, LOG2E = 1.4426950408889634f;
  float m = -INFINITY, l = 0.f, acc[8];
#pragma unroll
  for (int k = 0; k < 8; ++k) acc[k] = 0.f;
#define PROCESS(BUF) do { float s_[NS]; float mn_ = m; \
    _Pragma("unroll") for (int t_ = 0; t_ < NS; ++t_) { float d_ = dot2((BUF)[t_][0], q[0], 0.f); d_ = dot2((BUF)[t_][1], q[1], d_); d_ = dot2((BUF)[t_][2], q[2], d_); d_ = dot2((BUF)[t_][3], q[3], d_); \
      d_ = row16_sum(d_); s_[t_] = rbf(rbf(d_) * SCALE) * LOG2E; mn_ = fmaxf(mn_, s_[t_]); } \
    const float al_ = __builtin_amdgcn_exp2f(m - mn_); m = mn_; l *= al_; \
    _Pragma("unroll") for (int k_ = 0; k_ < 8; ++k_) acc[k_] *= al_; \
    _Pragma("unroll") for (int t_ = 0; t_ < NS; ++t_) { const float p_ = __builtin_amdgcn_exp2f(s_[t_] - mn_); l += p_; \
      _Pragma("unroll") for (int k_ = 0; k_ < 4; ++k_) { acc[2 * k_] = fmaf(p_, bf_lo((BUF)[NS + t_][k_]), acc[2 * k_]); acc[2 * k_ + 1] = fmaf(p_, bf_hi((BUF)[NS + t_][k_]), acc[2 * k_ + 1]); } } \
    asm volatile("" : "+v"(acc[0]), "+v"(acc[1]), "+v"(acc[2]), "+v"(acc[3]), "+v"(acc[4]), "+v"(acc[5]), "+v"(acc[6]), "+v"(acc[7]), "+v"(m), "+v"(l)); } while (0)

  if (wave < 4) {
#pragma unroll
    for (int i = 0; i < NPRE; ++i) { PROCESS(E + 4 * i); SB(); }
  }

  u32x4 O[16];
  u32x4 F[9], hg;
  float xo = 0.f;
  const __hip_bfloat16* wop = a.wo + (size_t)(h * DH + 16 * wave) * DM + 8 * lane;
  const int dr0 = DROWS * (rid - NG);
  const unsigned hoff = WS_H + 16 * (unsigned)min(dr0 + wave + 8 * min(lane, 8), FF - 1);
  const unsigned fdoff = WS_FD + (unsigned)(((lane >> 3) * 2 + ((lane >> 2) & 1)) * DM + 8 * rid + 2 * (lane & 3)) * 8;
  const bool l2 = (lane >> 3) + 16 < ND;
  const unsigned fdo2 = l2 ? fdoff + 16 * (2 * DM * 8) : fdoff;
#define LAST_ARRIVER() ({ unsigned arr_ = 0; asm volatile("s_waitcnt lgkmcnt(0)" ::: "memory"); \
      if (lane == 0) arr_ = __hip_atomic_fetch_add(reinterpret_cast<unsigned*>(lds) + L_CNT + 1, 1u, __ATOMIC_RELAXED, __HIP_MEMORY_SCOPE_WORKGROUP); \
      arr_ = __builtin_amdgcn_readfirstlane(arr_); asm volatile("" ::: "memory"); arr_ == NW - 1; })
#pragma unroll
  for (int i = 0; i < 16; ++i) {
    u32x4* buf = (i & 1) ? B : A;
    PROCESS(buf); SB(); O[i] = ld_nt(wop + (size_t)i * DM); SB(); ISSUE(buf, ts + T_STAT + NW * (i + 2) + wave);
    if (ROLE == 1 && i < 4) {
      const size_t o = (size_t)(64 * wave + 16 * i + (lane >> 2)) * FF + 32 * rid + 8 * (lane & 3);
      F[i] = ld_nt(a.wg + o); F[4 + i] = ld_nt(a.wu + o);
    }
    if (ROLE == 2 && i >= 2 && i < 7) {
#pragma unroll
      for (int k = 2 * (i - 2); k < 2 * (i - 2) + 2; ++k) if (k < 9) F[k] = ld_nt(a.wd + (size_t)min(dr0 + wave + 8 * k, FF - 1) * DM + 8 * lane);
    }
    if (ROLE == 2 && i == 12) hg = ld_sc1(wsr, hoff);
    if (ROLE == 3 && i == 13) {
      xo = __bfloat162float(a.x[((tid >> 3) & 1) * DM + 8 * rid + (tid & 7)]);
    }
    SB();
    if (ROLE == 1 && i == 5) {
      float v[32];
#pragma unroll
      for (int k = 0; k < 32; ++k) v[k] = 0.f;
#pragma unroll
      for (int ii = 0; ii < 4; ++ii) {
        const float xa = lds[L_XS + 64 * wave + 16 * ii + (lane >> 2)], xb = lds[L_XS + DM + 64 * wave + 16 * ii + (lane >> 2)];
#pragma unroll
        for (int k = 0; k < 4; ++k) {
          const float gl = bf_lo(F[ii][k]), gh = bf_hi(F[ii][k]), ul = bf_lo(F[4 + ii][k]), uh = bf_hi(F[4 + ii][k]);
          v[2 * k] = fmaf(xa, gl, v[2 * k]); v[2 * k + 1] = fmaf(xa, gh, v[2 * k + 1]);
          v[8 + 2 * k] = fmaf(xb, gl, v[8 + 2 * k]); v[8 + 2 * k + 1] = fmaf(xb, gh, v[8 + 2 * k + 1]);
          v[16 + 2 * k] = fmaf(xa, ul, v[16 + 2 * k]); v[16 + 2 * k + 1] = fmaf(xa, uh, v[16 + 2 * k + 1]);
          v[24 + 2 * k] = fmaf(xb, ul, v[24 + 2 * k]); v[24 + 2 * k + 1] = fmaf(xb, uh, v[24 + 2 * k + 1]);
        }
      }
#pragma unroll
      for (int k = 0; k < 16; ++k) v[k] = swap32_add(v[k], v[k + 16]);
#pragma unroll
      for (int k = 0; k < 8; ++k) v[k] = swap16_add(v[k], v[k + 8]);
#pragma unroll
      for (int k = 0; k < 4; ++k) { const float t1 = v[k] + dpp<ROR8>(v[k]), t2 = v[k + 4] + dpp<ROR8>(v[k + 4]); v[k] = (lane & 8) ? t2 : t1; }
      *reinterpret_cast<f32x4*>(lds + L_FR + wave * 256 + ((lane >> 2) & 1) * 128 + (lane >> 4) * 32 + 8 * (lane & 3) + 4 * ((lane >> 3) & 1)) = f32x4{v[0], v[1], v[2], v[3]};
      if (LAST_ARRIVER() && lane < 32) {
        float hv[2];
#pragma unroll
        for (int bb = 0; bb < 2; ++bb) {
          float g = 0.f, u = 0.f;
#pragma unroll
          for (int w = 0; w < 2 * NW; ++w) { g += lds[L_FR + w * 128 + bb * 32 + lane]; u += lds[L_FR + w * 128 + 64 + bb * 32 + lane]; }
          g = rbf(g); u = rbf(u);
          const float ge = rbf(0.5f * g * (1.f + tanhf(0.7978845608028654f * (g + 0.044715f * g * g * g))));
          hv[bb] = rbf(ge * u);
        }
        st_sc1(wsr, WS_H + 16 * (32 * rid + lane), u32x4{__float_as_uint(hv[0]), T, __float_as_uint(hv[1]), T});
      }
      SB();
    }
    if (ROLE == 2 && i == 14) {
      if (!__all(hg[1] == T && hg[3] == T)) {
        const unsigned long long t0 = __builtin_amdgcn_s_memrealtime();
        do { __builtin_amdgcn_s_sleep(8); hg = ld_sc1(wsr, hoff); } while (!__all(hg[1] == T && hg[3] == T) && __builtin_amdgcn_s_memrealtime() - t0 < 2000000ull);
      }
      const bool live = wave + 8 * lane < DROWS && dr0 + wave + 8 * lane < FF;
      const int ha_ = live ? (int)hg[0] : 0, hb_ = live ? (int)hg[2] : 0;
      float v[16];
#pragma unroll
      for (int k = 0; k < 16; ++k) v[k] = 0.f;
#pragma unroll
      for (int k9 = 0; k9 < 9; ++k9) {
        const float ha = __builtin_bit_cast(float, __builtin_amdgcn_readlane(ha_, k9)), hb = __builtin_bit_cast(float, __builtin_amdgcn_readlane(hb_, k9));
#pragma unroll
        for (int k = 0; k < 4; ++k) {
          const float lo = bf_lo(F[k9][k]), hi = bf_hi(F[k9][k]);
          v[2 * k] = fmaf(ha, lo, v[2 * k]); v[2 * k + 1] = fmaf(ha, hi, v[2 * k + 1]);
          v[8 + 2 * k] = fmaf(hb, lo, v[8 + 2 * k]); v[8 + 2 * k + 1] = fmaf(hb, hi, v[8 + 2 * k + 1]);
        }
      }
#pragma unroll
      for (int k = 0; k < 4; ++k)
        *reinterpret_cast<f32x4*>(lds + L_FR + wave * 1024 + (k >> 1) * DM + 8 * lane + 4 * (k & 1)) = f32x4{v[4 * k], v[4 * k + 1], v[4 * k + 2], v[4 * k + 3]};
      if (LAST_ARRIVER()) {
#pragma unroll
        for (int k = 0; k < 8; ++k) {
          f32x2 sacc = {0.f, 0.f};
#pragma unroll
          for (int w = 0; w < NW; ++w) sacc += *reinterpret_cast<const f32x2*>(lds + L_FR + w * 1024 + 2 * (lane + 64 * k));
          st_sc1(wsr, WS_FD + (unsigned)(rid - NG) * (2 * DM * 8) + 16 * (lane + 64 * k), u32x4{__float_as_uint(sacc[0]), T, __float_as_uint(sacc[1]), T});
        }
      }
      SB();
    }
  }
  unsigned* cnt = reinterpret_cast<unsigned*>(lds) + L_CNT;
  unsigned tk = 0;
  if (lane == 0) tk = __hip_atomic_fetch_add(cnt, 2u, __ATOMIC_RELAXED, __HIP_MEMORY_SCOPE_WORKGROUP);
  f32x2 OF[16][4];
#pragma unroll
  for (int t = 0; t < 16; ++t)
#pragma unroll
    for (int k = 0; k < 4; ++k) { OF[t][k] = f32x2{bf_lo(O[t][k]), bf_hi(O[t][k])}; asm volatile("" : "+v"(OF[t][k])); }
  tk = __builtin_amdgcn_readfirstlane(tk);
#pragma unroll 1
  while (tk < tend) {
    unsigned nx = 0;
    if (lane == 0) nx = __hip_atomic_fetch_add(cnt, 2u, __ATOMIC_RELAXED, __HIP_MEMORY_SCOPE_WORKGROUP);
    PROCESS(A); SB(); ISSUE(A, tk); SB();
    PROCESS(B); SB(); ISSUE(B, tk + 1); SB();
    tk = __builtin_amdgcn_readfirstlane(nx);
  }
  PROCESS(A);
  PROCESS(B);
  STAMP_A(5);

  {
    const float mw = xrow_max(m), sc = __builtin_amdgcn_exp2f(m - mw);
    l = xrow_sum(l * sc);
#pragma unroll
    for (int k = 0; k < 8; ++k) acc[k] = xrow_sum(acc[k] * sc);
    float* rec = lds + L_MRG + wave * 132;
    if (r == 0) {
      *reinterpret_cast<f32x4*>(rec + 4 + 8 * c) = f32x4{acc[0], acc[1], acc[2], acc[3]};
      *reinterpret_cast<f32x4*>(rec + 4 + 8 * c + 4) = f32x4{acc[4], acc[5], acc[6], acc[7]};
      if (c == 0) { rec[0] = mw; rec[1] = l; }
    }
  }
  __syncthreads();
  const int e = (b * NH + h) * NSPLIT + split;
  {
    const float* mrg = lds + L_MRG;
    const int sw = lane & 7, g = lane >> 3;
    const float mc = mrg[sw * 132], lc = mrg[sw * 132 + 1];
    float M = mc;
    M = fmaxf(M, dpp<XOR1>(M)); M = fmaxf(M, dpp<XOR2>(M)); M = fmaxf(M, dpp<HMIR>(M));
    const float wgt = __builtin_amdgcn_exp2f(mc - M);
    const f32x2 av = *reinterpret_cast<const f32x2*>(mrg + sw * 132 + 4 + 16 * wave + 2 * g);
    float o0 = av[0] * wgt, o1 = av[1] * wgt, L = lc * wgt;
    o0 += dpp<XOR1>(o0); o0 += dpp<XOR2>(o0); o0 += dpp<HMIR>(o0);
    o1 += dpp<XOR1>(o1); o1 += dpp<XOR2>(o1); o1 += dpp<HMIR>(o1);
    L += dpp<XOR1>(L); L += dpp<XOR2>(L); L += dpp<HMIR>(L);
    f32x2 y[4];
#pragma unroll
    for (int k = 0; k < 4; ++k) y[k] = f32x2{0.f, 0.f};
#pragma unroll
    for (int t = 0; t < 16; ++t) {
      const float ov = __builtin_bit_cast(float, __builtin_amdgcn_readlane(__builtin_bit_cast(int, (t & 1) ? o1 : o0), 8 * (t >> 1)));
#pragma unroll
      for (int k = 0; k < 4; ++k) y[k] = __builtin_elementwise_fma(f32x2{ov, ov}, OF[t][k], y[k]);
    }
    float* yr = lds + L_YR;
    *reinterpret_cast<f32x4*>(yr + wave * DM + 8 * lane) = f32x4{y[0][0], y[0][1], y[1][0], y[1][1]};
    *reinterpret_cast<f32x4*>(yr + wave * DM + 8 * lane + 4) = f32x4{y[2][0], y[2][1], y[3][0], y[3][1]};
    if (tid == 448) st_sc1(wsr, WS_ML + 16 * e, u32x4{__float_as_uint(M), T, __float_as_uint(L), T});
    __syncthreads();
    if (wave < 4) {
      f32x2 s = {0.f, 0.f};
#pragma unroll
      for (int w = 0; w < NW; ++w) s += *reinterpret_cast<const f32x2*>(yr + w * DM + 2 * tid);
      st_sc1(wsr, WS_Y + (unsigned)e * (DM * 8) + 16 * tid, u32x4{__float_as_uint(s[0]), T, __float_as_uint(s[1]), T});
    }
  }
  STAMP_A(6);
  if (ROLE == 3) {
    const unsigned pe = tid >> 1, offy = WS_Y + pe * (DM * 8) + (8 * rid + 4 * (tid & 1)) * 8, offm = WS_ML + 16 * pe;
    u32x4 y0 = ld_sc1(wsr, offy), y1 = ld_sc1(wsr, offy + 16), ml = ld_sc1(wsr, offm);
    if (wave == 7) {
      u32x4 p0 = ld_sc1(wsr, fdoff), p1 = ld_sc1(wsr, fdoff + 8 * (2 * DM * 8)), p2 = ld_sc1(wsr, fdo2);
      bool ok = p0[1] == T && p0[3] == T && p1[1] == T && p1[3] == T && p2[1] == T && p2[3] == T;
      if (!__all(ok)) {
        const unsigned long long t0 = __builtin_amdgcn_s_memrealtime();
        do {
          __builtin_amdgcn_s_sleep(2);
          p0 = ld_sc1(wsr, fdoff); p1 = ld_sc1(wsr, fdoff + 8 * (2 * DM * 8)); p2 = ld_sc1(wsr, fdo2);
          ok = p0[1] == T && p0[3] == T && p1[1] == T && p1[3] == T && p2[1] == T && p2[3] == T;
        } while (!__all(ok) && __builtin_amdgcn_s_memrealtime() - t0 < 2000000ull);
      }
      float f0 = __uint_as_float(p0[0]) + __uint_as_float(p1[0]) + (l2 ? __uint_as_float(p2[0]) : 0.f);
      float f1 = __uint_as_float(p0[2]) + __uint_as_float(p1[2]) + (l2 ? __uint_as_float(p2[2]) : 0.f);
      f0 += dpp<ROR8>(f0); f1 += dpp<ROR8>(f1);
      f0 = xrow_sum(f0); f1 = xrow_sum(f1);
      f0 = rbf(f0); f1 = rbf(f1);
      if (lane < 8) *reinterpret_cast<f32x2*>(lds + L_FIN + 64 + 2 * lane) = f32x2{rbf(f0 / (1.f + __expf(-f0))), rbf(f1 / (1.f + __expf(-f1)))};
    }
    bool ok0 = y0[1] == T && y0[3] == T, ok1 = y1[1] == T && y1[3] == T, okm = ml[1] == T && ml[3] == T;
    if (!__all(ok0 && ok1 && okm)) {
      const unsigned long long t0 = __builtin_amdgcn_s_memrealtime();
      do {
        __builtin_amdgcn_s_sleep(2);
        if (!ok0) { y0 = ld_sc1(wsr, offy); }
        if (!ok1) { y1 = ld_sc1(wsr, offy + 16); }
        if (!okm) { ml = ld_sc1(wsr, offm); }
        ok0 = y0[1] == T && y0[3] == T; ok1 = y1[1] == T && y1[3] == T; okm = ml[1] == T && ml[3] == T;
      } while (!__all(ok0 && ok1 && okm) && __builtin_amdgcn_s_memrealtime() - t0 < 2000000ull);
    }
    const float mm = __uint_as_float(ml[0]), ll = __uint_as_float(ml[2]);
    float M = fmaxf(mm, dpp<XOR2>(mm)); M = fmaxf(M, dpp<HMIR>(M));
    const float ew = __builtin_amdgcn_exp2f(mm - M);
    float L = ew * ll; L += dpp<XOR2>(L); L += dpp<HMIR>(L);
    const float wgt = ew * __builtin_amdgcn_rcpf(L);
    float v0 = __uint_as_float(y0[0]) * wgt, v1 = __uint_as_float(y0[2]) * wgt, v2 = __uint_as_float(y1[0]) * wgt, v3 = __uint_as_float(y1[2]) * wgt;
    v0 = swap32_add(v0, v2); v1 = swap32_add(v1, v3);
    v0 = swap16_add(v0, v1);
    v0 += dpp<ROR8>(v0); v0 += dpp<ROR4>(v0); v0 += dpp<XOR2>(v0);
    if ((lane & 14) == 0) lds[L_FIN + wave * 8 + 4 * (lane & 1) + (lane >> 4)] = v0;
    __syncthreads();
    if (tid < 16) {
      const int bb = tid >> 3, k = tid & 7;
      const float sv = rbf(lds[L_FIN + (4 * bb) * 8 + k] + lds[L_FIN + (4 * bb + 1) * 8 + k] + lds[L_FIN + (4 * bb + 2) * 8 + k] + lds[L_FIN + (4 * bb + 3) * 8 + k]);
      const float g = lds[L_FIN + 64 + tid];
      a.out[bb * DM + 8 * rid + k] = __float2bfloat16(xo + rbf(sv * g));
    }
    if (rid == 0 && tid == 0) __hip_atomic_store(reinterpret_cast<unsigned*>(ws + WS_EPOCH), T, __ATOMIC_RELAXED, __HIP_MEMORY_SCOPE_AGENT);
  }
  STAMP_A(7);
}

__global__ void __launch_bounds__(NTHR) fused_kernel(Args a) {
  __shared__ __attribute__((aligned(16))) float lds[L_END];
  const int xcd = blockIdx.x & 7, j = blockIdx.x >> 3;
  const int g = (j >> 4) * NH + xcd * 4 + (j & 3), split = (j >> 2) & 3;
  if (split == 0) { if (g < NG) body<1>(a, lds, g); else body<2>(a, lds, g); }
  else if (split == 1) body<3>(a, lds, g);
  else body<0>(a, lds, g);
}

extern "C" void kernel_launch(void* const* d_in, const int* in_sizes, int n_in, void* d_out, int out_size, void* d_ws, size_t ws_size,
                              hipStream_t stream) {
  Args a;
  a.x = (const __hip_bfloat16*)d_in[0]; a.Kc = (const __hip_bfloat16*)d_in[1]; a.Vc = (const __hip_bfloat16*)d_in[2];
  a.wq = (const __hip_bfloat16*)d_in[3]; a.wo = (const __hip_bfloat16*)d_in[4]; a.wg = (const __hip_bfloat16*)d_in[6];
  a.wu = (const __hip_bfloat16*)d_in[7]; a.wd = (const __hip_bfloat16*)d_in[8];
  a.out = (__hip_bfloat16*)d_out; a.ws = (unsigned char*)d_ws;
  fused_kernel<<<256, NTHR, 0, stream>>>(a);
}
```

```cpp
#include <hip/hip_runtime.h>
#include <hip/hip_bf16.h>
#include <cstdint>

typedef unsigned int u32;
using u32x4 = __attribute__((ext_vector_type(4))) unsigned;
using f32x4 = __attribute__((ext_vector_type(4))) float;
using f32x2 = __attribute__((ext_vector_type(2))) float;
using bf16x2 = __attribute__((ext_vector_type(2))) __bf16;

constexpr int NB = 2, DM = 512, NH = 32, SK = 8192, DH = 128, FF = 1376;
constexpr int NSPLIT = 4, KEYS_WG = SK / NSPLIT;
constexpr int NW = 8, NTHR = NW * 64, NS = 2;
constexpr int TKEYS = 4 * NS;
constexpr int NT = SK / TKEYS;
#define CFG_NPRE 4
#define CFG_DG 6
#define CFG_DD 4
constexpr int NPRE = CFG_NPRE;
constexpr int T_STAT = 4 * NPRE;
constexpr int NSTATIC = T_STAT + 18 * NW;
constexpr int NG = FF / 32, ND = 21, DROWS = 66;
constexpr size_t WS_EPOCH = 0;
constexpr size_t WS_H = 256;
constexpr size_t WS_ML = WS_H + 1376 * 16;
constexpr size_t WS_FD = WS_ML + 256 * 16;
constexpr size_t WS_Y = WS_FD + ND * 2 * DM * 8;
#define STAMP_A(k)
#define KPLAIN 0
#define VPLAIN 1
#define SB() __builtin_amdgcn_sched_barrier(0)

__device__ __forceinline__ float dot2(unsigned k, unsigned q, float acc) {
  return __builtin_amdgcn_fdot2_f32_bf16(__builtin_bit_cast(bf16x2, k), __builtin_bit_cast(bf16x2, q), acc, false);
}
template <int CTRL> __device__ __forceinline__ float dpp(float x) {
  return __builtin_bit_cast(float, __builtin_amdgcn_mov_dpp(__builtin_bit_cast(int, x), CTRL, 0xf, 0xf, true));
}
constexpr int XOR1 = 0xB1, XOR2 = 0x4E, HMIR = 0x141, ROR8 = 0x128, ROR4 = 0x124;
__device__ __forceinline__ float row16_sum(float x) {
  x += dpp<XOR1>(x); x += dpp<XOR2>(x); x += dpp<HMIR>(x); x += dpp<ROR8>(x); return x;
}
__device__ __forceinline__ float swap16_add(float a, float b) {
  auto s = __builtin_amdgcn_permlane16_swap(__float_as_uint(a), __float_as_uint(b), false, false);
  return __uint_as_float(s[0]) + __uint_as_float(s[1]);
}
__device__ __forceinline__ float swap32_add(float a, float b) {
  auto s = __builtin_amdgcn_permlane32_swap(__float_as_uint(a), __float_as_uint(b), false, false);
  return __uint_as_float(s[0]) + __uint_as_float(s[1]);
}
__device__ __forceinline__ float xrow_sum(float x) { x = swap16_add(x, x); return swap32_add(x, x); }
__device__ __forceinline__ float xrow_max(float x) {
  auto s = __builtin_amdgcn_permlane16_swap(__float_as_uint(x), __float_as_uint(x), false, false);
  x = fmaxf(__uint_as_float(s[0]), __uint_as_float(s[1]));
  auto t = __builtin_amdgcn_permlane32_swap(__float_as_uint(x), __float_as_uint(x), false, false);
  return fmaxf(__uint_as_float(t[0]), __uint_as_float(t[1]));
}
__device__ __forceinline__ float wave_sum(float x) { return xrow_sum(row16_sum(x)); }
__device__ __forceinline__ float treduce32(float (&v)[32], int lane) {
#pragma unroll
  for (int i = 0; i < 16; ++i) v[i] = swap32_add(v[i], v[i + 16]);
#pragma unroll
  for (int i = 0; i < 8; ++i) v[i] = swap16_add(v[i], v[i + 8]);
  const bool b8 = lane & 8, b4 = lane & 4, b2 = lane & 2;
#pragma unroll
  for (int i = 0; i < 4; ++i) { const float t1 = v[i] + dpp<ROR8>(v[i]), t2 = v[i + 4] + dpp<ROR8>(v[i + 4]); v[i] = b8 ? t2 : t1; }
#pragma unroll
  for (int i = 0; i < 2; ++i) { const float t1 = v[i] + dpp<HMIR>(v[i]), t2 = v[i + 2] + dpp<HMIR>(v[i + 2]); v[i] = b4 ? t2 : t1; }
  { const float t1 = v[0] + dpp<XOR2>(v[0]), t2 = v[1] + dpp<XOR2>(v[1]); v[0] = b2 ? t2 : t1; }
  return v[0] + dpp<XOR1>(v[0]);
}
__device__ __forceinline__ float treduce16(float (&v)[16], int lane) {
#pragma unroll
  for (int i = 0; i < 8; ++i) v[i] = swap32_add(v[i], v[i + 8]);
#pragma unroll
  for (int i = 0; i < 4; ++i) v[i] = swap16_add(v[i], v[i + 4]);
  const bool b8 = lane & 8, b4 = lane & 4;
#pragma unroll
  for (int i = 0; i < 2; ++i) { const float t1 = v[i] + dpp<ROR8>(v[i]), t2 = v[i + 2] + dpp<ROR8>(v[i + 2]); v[i] = b8 ? t2 : t1; }
  { const float t1 = v[0] + dpp<HMIR>(v[0]), t2 = v[1] + dpp<HMIR>(v[1]); v[0] = b4 ? t2 : t1; }
  v[0] += dpp<XOR2>(v[0]);
  return v[0] + dpp<XOR1>(v[0]);
}
__device__ __forceinline__ float bf_lo(unsigned u) { return __uint_as_float(u << 16); }
__device__ __forceinline__ float bf_hi(unsigned u) { return __uint_as_float(u & 0xffff0000u); }
__device__ __forceinline__ float rbf(float x) { return (float)(__bf16)x; }
__device__ __forceinline__ unsigned pk_bf(float lo, float hi) { bf16x2 v = {(__bf16)lo, (__bf16)hi}; return __builtin_bit_cast(unsigned, v); }
__device__ __forceinline__ u32x4 ld_nt(const void* p) { return __builtin_nontemporal_load(reinterpret_cast<const u32x4*>(p)); }
__device__ __forceinline__ u32x4 ld(const void* p) { return *reinterpret_cast<const u32x4*>(p); }
typedef __attribute__((address_space(8))) void* rsrc_t_;
__device__ __forceinline__ void st_sc1(__amdgpu_buffer_rsrc_t rs, unsigned off, u32x4 v) { __builtin_amdgcn_raw_buffer_store_b128(v, rs, off, 0, 16); }
__device__ __forceinline__ u32x4 ld_sc1(__amdgpu_buffer_rsrc_t rs, unsigned off) { return __builtin_amdgcn_raw_buffer_load_b128(rs, off, 0, 16); }

__device__ __forceinline__ unsigned long long atomic_add_async(unsigned long long* p, unsigned long long v) {
  unsigned long long r, sv;
  asm volatile("s_mov_b64 %1, exec\n\ts_mov_b64 exec, 1\n\tglobal_atomic_add_x2 %0, %2, %3, off sc0\n\ts_mov_b64 exec, %1"
               : "=&v"(r), "=&s"(sv) : "v"(p), "v"(v) : "memory");
  return r;
}
__device__ __forceinline__ void layernorm8(u32x4 xr, float (&xn)[8]) {
  float v[8];
#pragma unroll
  for (int k = 0; k < 4; ++k) { v[2 * k] = bf_lo(xr[k]); v[2 * k + 1] = bf_hi(xr[k]); }
  float s = 0.f;
#pragma unroll
  for (int k = 0; k < 8; ++k) s += v[k];
  const float mean = wave_sum(s) * (1.f / DM);
  float q = 0.f;
#pragma unroll
  for (int k = 0; k < 8; ++k) { const float d = v[k] - mean; q = fmaf(d, d, q); }
  const float var = wave_sum(q) * (1.f / DM);
  const float mb = rbf(mean), den = rbf(__builtin_amdgcn_sqrtf(rbf(rbf(var) + 1e-5f))), rden = __builtin_amdgcn_rcpf(den);
#pragma unroll
  for (int k = 0; k < 8; ++k) xn[k] = rbf(rbf(v[k] - mb) * rden);
}

constexpr int L_YR = 0;
constexpr int L_XP = 0;
constexpr int L_QP = L_XP + 4 * 256;
constexpr int L_MRG = NW * 512;
constexpr int L_XS = L_MRG + NW * 132;
constexpr int L_FIN = L_XS + 1024;
constexpr int L_CNT = L_FIN + 64 + 16;
constexpr int L_FR = L_CNT + 4;
constexpr int L_END = L_FR + NW * 1024;

struct Args {
  const __hip_bfloat16 *x, *Kc, *Vc, *wq, *wo, *wg, *wu, *wd;
  __hip_bfloat16* out;
  unsigned char* ws;
};

template <int ROLE>
__device__ __forceinline__ void body(const Args& a, float* lds, const int rid  ) {
  unsigned char* ws = a.ws;
  const int tid = threadIdx.x, lane = tid & 63, wave = __builtin_amdgcn_readfirstlane(tid >> 6), r = lane >> 4, c = lane & 15;
  const int bid = blockIdx.x, xcd = bid & 7, j = bid >> 3;
  const int h = xcd * 4 + (j & 3), b = j >> 4, split = (j >> 2) & 3;
  const __amdgpu_buffer_rsrc_t wsr = __builtin_amdgcn_make_buffer_rsrc(ws, 0, 4u << 20, 0x00020000);
  STAMP_A(0);

  const size_t kvbase = ((size_t)(b * NH + h) * SK + r) * DH + 8 * c;
  const int dd = rid < NG ? CFG_DG : CFG_DD;
  const int ts = split == 0 ? 0 : 256 - 3 * dd + (split - 1) * (256 + dd);
  const unsigned tend = split == 0 ? 256 - 3 * dd : ts + 256 + dd;
  const __hip_bfloat16* kp = a.Kc + kvbase;
  const __hip_bfloat16* vp = a.Vc + kvbase;
#define ISSUE(BUF, T) do { const size_t o_ = (size_t)(T) * (TKEYS * DH); \
    _Pragma("unroll") for (int t_ = 0; t_ < NS; ++t_) (BUF)[t_] = KPLAIN ? ld(kp + o_ + 4 * t_ * DH) : ld_nt(kp + o_ + 4 * t_ * DH); \
    _Pragma("unroll") for (int t_ = 0; t_ < NS; ++t_) (BUF)[NS + t_] = VPLAIN ? ld(vp + o_ + 4 * t_ * DH) : ld_nt(vp + o_ + 4 * t_ * DH); } while (0)

  u32x4 E[32];
  unsigned epoch = 0;
  if (wave < 4) {
    u32x4 xr;
    if (ROLE == 1) xr = ld(a.x + (wave & 1) * DM + 8 * lane);
    epoch = __hip_atomic_load(reinterpret_cast<const unsigned*>(ws + WS_EPOCH), __ATOMIC_RELAXED, __HIP_MEMORY_SCOPE_AGENT);
    if (tid == 0) { reinterpret_cast<unsigned*>(lds)[L_CNT] = ts + NSTATIC; reinterpret_cast<unsigned*>(lds)[L_CNT + 1] = 0; }
    SB();
    __builtin_amdgcn_s_barrier();
    SB();
#pragma unroll
    for (int i = 0; i < NPRE; ++i) ISSUE(E + 4 * i, ts + 4 * i + wave);
    SB();
    if (ROLE == 1) {
      float n0[8];
      layernorm8(xr, n0);
      if (wave < 2) {
        *reinterpret_cast<f32x4*>(lds + L_XS + wave * DM + 8 * lane) = f32x4{n0[0], n0[1], n0[2], n0[3]};
        *reinterpret_cast<f32x4*>(lds + L_XS + wave * DM + 8 * lane + 4) = f32x4{n0[4], n0[5], n0[6], n0[7]};
      }
    }
    STAMP_A(1); STAMP_A(2);
  } else {
    const int qw = wave - 4;
    const u32x4 x0 = ld(a.x + b * DM + 8 * lane);
    SB();
    {
      const __hip_bfloat16* p = a.wq + (size_t)(128 * qw + r) * (NH * DH) + h * DH + 8 * c;
#pragma unroll
      for (int i = 0; i < 32; ++i) E[i] = ld_nt(p + (size_t)(4 * i) * (NH * DH));
    }
    epoch = __hip_atomic_load(reinterpret_cast<const unsigned*>(ws + WS_EPOCH), __ATOMIC_RELAXED, __HIP_MEMORY_SCOPE_AGENT);
    SB();
    __builtin_amdgcn_s_barrier();
    SB();
    STAMP_A(1);
    {
      float n0[8];
      layernorm8(x0, n0);
      u32x4 pk;
#pragma unroll
      for (int k = 0; k < 4; ++k) pk[k] = pk_bf(n0[k], n0[k + 4]);
      *reinterpret_cast<u32x4*>(lds + L_XP + qw * 256 + 4 * lane) = pk;
    }
    float qa[8];
#pragma unroll
    for (int k = 0; k < 8; ++k) qa[k] = 0.f;
    const unsigned* xp = reinterpret_cast<const unsigned*>(lds + L_XP + qw * 256 + 64 * qw + r);
#pragma unroll
    for (int p = 0; p < 16; ++p) {
      const unsigned xv = xp[4 * p];
#pragma unroll
      for (int k = 0; k < 4; ++k) {
        const unsigned lo = __builtin_amdgcn_perm(E[2 * p + 1][k], E[2 * p][k], 0x05040100u);
        const unsigned hi = __builtin_amdgcn_perm(E[2 * p + 1][k], E[2 * p][k], 0x07060302u);
        qa[2 * k] = dot2(lo, xv, qa[2 * k]); qa[2 * k + 1] = dot2(hi, xv, qa[2 * k + 1]);
      }
    }
#pragma unroll
    for (int k = 0; k < 8; ++k) qa[k] = xrow_sum(qa[k]);
    if (r == 0) {
      *reinterpret_cast<f32x4*>(lds + L_QP + qw * DH + 8 * c) = f32x4{qa[0], qa[1], qa[2], qa[3]};
      *reinterpret_cast<f32x4*>(lds + L_QP + qw * DH + 8 * c + 4) = f32x4{qa[4], qa[5], qa[6], qa[7]};
    }
    STAMP_A(2);
  }
  __syncthreads();
  STAMP_A(3);
  u32x4 A[2 * NS], B[2 * NS];
  ISSUE(A, ts + T_STAT + wave);
  ISSUE(B, ts + T_STAT + NW + wave);
  SB();
  u32x4 q;
  {
    const f32x4 s0 = *reinterpret_cast<const f32x4*>(lds + L_QP + r * DH + 8 * c);
    const f32x4 s1 = *reinterpret_cast<const f32x4*>(lds + L_QP + r * DH + 8 * c + 4);
    float qa[8] = {s0[0], s0[1], s0[2], s0[3], s1[0], s1[1], s1[2], s1[3]};
#pragma unroll
    for (int k = 0; k < 8; ++k) qa[k] = xrow_sum(qa[k]);
#pragma unroll
    for (int k = 0; k < 4; ++k) q[k] = pk_bf(qa[2 * k], qa[2 * k + 1]);
  }
  const unsigned T = __builtin_amdgcn_readfirstlane(epoch) + 1u;
  STAMP_A(4);

  constexpr float SCALE = 0.08838834764831845f, LOG2E = 1.4426950408889634f;
  float m = -INFINITY, l = 0.f, acc[8];
#pragma unroll
  for (int k = 0; k < 8; ++k) acc[k] = 0.f;
#define PROCESS(BUF) do { float s_[NS]; float mn_ = m; \
    _Pragma("unroll") for (int t_ = 0; t_ < NS; ++t_) { float d_ = dot2((BUF)[t_][0], q[0], 0.f); d_ = dot2((BUF)[t_][1], q[1], d_); d_ = dot2((BUF)[t_][2], q[2], d_); d_ = dot2((BUF)[t_][3], q[3], d_); \
      d_ = row16_sum(d_); s_[t_] = rbf(rbf(d_) * SCALE) * LOG2E; mn_ = fmaxf(mn_, s_[t_]); } \
    const float al_ = __builtin_amdgcn_exp2f(m - mn_); m = mn_; l *= al_; \
    _Pragma("unroll") for (int k_ = 0; k_ < 8; ++k_) acc[k_] *= al_; \
    _Pragma("unroll") for (int t_ = 0; t_ < NS; ++t_) { const float p_ = __builtin_amdgcn_exp2f(s_[t_] - mn_); l += p_; \
      _Pragma("unroll") for (int k_ = 0; k_ < 4; ++k_) { acc[2 * k_] = fmaf(p_, bf_lo((BUF)[NS + t_][k_]), acc[2 * k_]); acc[2 * k_ + 1] = fmaf(p_, bf_hi((BUF)[NS + t_][k_]), acc[2 * k_ + 1]); } } \
    asm volatile("" : "+v"(acc[0]), "+v"(acc[1]), "+v"(acc[2]), "+v"(acc[3]), "+v"(acc[4]), "+v"(acc[5]), "+v"(acc[6]), "+v"(acc[7]), "+v"(m), "+v"(l)); } while (0)

  if (wave < 4) {
#pragma unroll
    for (int i = 0; i < NPRE; ++i) { PROCESS(E + 4 * i); SB(); }
  }

  u32x4 O[16];
  u32x4 F[9], hg;
  float xo = 0.f;
  const __hip_bfloat16* wop = a.wo + (size_t)(h * DH + 16 * wave) * DM + 8 * lane;
  const int dr0 = DROWS * (rid - NG);
  const unsigned hoff = WS_H + 16 * (unsigned)min(dr0 + wave + 8 * min(lane, 8), FF - 1);
  const unsigned fdoff = WS_FD + (unsigned)(((lane >> 3) * 2 + ((lane >> 2) & 1)) * DM + 8 * rid + 2 * (lane & 3)) * 8;
  const bool l2 = (lane >> 3) + 16 < ND;
  const unsigned fdo2 = l2 ? fdoff + 16 * (2 * DM * 8) : fdoff;
#define LAST_ARRIVER() ({ unsigned arr_ = 0; asm volatile("s_waitcnt lgkmcnt(0)" ::: "memory"); \
      if (lane == 0) arr_ = __hip_atomic_fetch_add(reinterpret_cast<unsigned*>(lds) + L_CNT + 1, 1u, __ATOMIC_RELAXED, __HIP_MEMORY_SCOPE_WORKGROUP); \
      arr_ = __builtin_amdgcn_readfirstlane(arr_); asm volatile("" ::: "memory"); arr_ == NW - 1; })
#pragma unroll
  for (int i = 0; i < 16; ++i) {
    u32x4* buf = (i & 1) ? B : A;
    PROCESS(buf); SB(); O[i] = ld_nt(wop + (size_t)i * DM); SB(); ISSUE(buf, ts + T_STAT + NW * (i + 2) + wave);
    if (ROLE == 1 && i < 4) {
      const size_t o = (size_t)(64 * wave + 16 * i + (lane >> 2)) * FF + 32 * rid + 8 * (lane & 3);
      F[i] = ld_nt(a.wg + o); F[4 + i] = ld_nt(a.wu + o);
    }
    if (ROLE == 2 && i >= 2 && i < 7) {
#pragma unroll
      for (int k = 2 * (i - 2); k < 2 * (i - 2) + 2; ++k) if (k < 9) F[k] = ld_nt(a.wd + (size_t)min(dr0 + wave + 8 * k, FF - 1) * DM + 8 * lane);
    }
    if (ROLE == 2 && i == 12) hg = ld_sc1(wsr, hoff);
    if (ROLE == 3 && i == 13) {
      xo = __bfloat162float(a.x[((tid >> 3) & 1) * DM + 8 * rid + (tid & 7)]);
    }
    SB();
    if (ROLE == 1 && i == 5) {
      float v[32];
#pragma unroll
      for (int k = 0; k < 32; ++k) v[k] = 0.f;
#pragma unroll
      for (int ii = 0; ii < 4; ++ii) {
        const float xa = lds[L_XS + 64 * wave + 16 * ii + (lane >> 2)], xb = lds[L_XS + DM + 64 * wave + 16 * ii + (lane >> 2)];
#pragma unroll
        for (int k = 0; k < 4; ++k) {
          const float gl = bf_lo(F[ii][k]), gh = bf_hi(F[ii][k]), ul = bf_lo(F[4 + ii][k]), uh = bf_hi(F[4 + ii][k]);
          v[2 * k] = fmaf(xa, gl, v[2 * k]); v[2 * k + 1] = fmaf(xa, gh, v[2 * k + 1]);
          v[8 + 2 * k] = fmaf(xb, gl, v[8 + 2 * k]); v[8 + 2 * k + 1] = fmaf(xb, gh, v[8 + 2 * k + 1]);
          v[16 + 2 * k] = fmaf(xa, ul, v[16 + 2 * k]); v[16 + 2 * k + 1] = fmaf(xa, uh, v[16 + 2 * k + 1]);
          v[24 + 2 * k] = fmaf(xb, ul, v[24 + 2 * k]); v[24 + 2 * k + 1] = fmaf(xb, uh, v[24 + 2 * k + 1]);
        }
      }
#pragma unroll
      for (int k = 0; k < 16; ++k) v[k] = swap32_add(v[k], v[k + 16]);
#pragma unroll
      for (int k = 0; k < 8; ++k) v[k] = swap16_add(v[k], v[k + 8]);
#pragma unroll
      for (int k = 0; k < 4; ++k) { const float t1 = v[k] + dpp<ROR8>(v[k]), t2 = v[k + 4] + dpp<ROR8>(v[k + 4]); v[k] = (lane & 8) ? t2 : t1; }
      *reinterpret_cast<f32x4*>(lds + L_FR + wave * 256 + ((lane >> 2) & 1) * 128 + (lane >> 4) * 32 + 8 * (lane & 3) + 4 * ((lane >> 3) & 1)) = f32x4{v[0], v[1], v[2], v[3]};
      if (LAST_ARRIVER() && lane < 32) {
        float hv[2];
#pragma unroll
        for (int bb = 0; bb < 2; ++bb) {
          float g = 0.f, u = 0.f;
#pragma unroll
          for (int w = 0; w < 2 * NW; ++w) { g += lds[L_FR + w * 128 + bb * 32 + lane]; u += lds[L_FR + w * 128 + 64 + bb * 32 + lane]; }
          g = rbf(g); u = rbf(u);
          const float ge = rbf(0.5f * g * (1.f + tanhf(0.7978845608028654f * (g + 0.044715f * g * g * g))));
          hv[bb] = rbf(ge * u);
        }
        st_sc1(wsr, WS_H + 16 * (32 * rid + lane), u32x4{__float_as_uint(hv[0]), T, __float_as_uint(hv[1]), T});
      }
      SB();
    }
    if (ROLE == 2 && i == 14) {
      if (!__all(hg[1] == T && hg[3] == T)) {
        const unsigned long long t0 = __builtin_amdgcn_s_memrealtime();
        do { __builtin_amdgcn_s_sleep(8); hg = ld_sc1(wsr, hoff); } while (!__all(hg[1] == T && hg[3] == T) && __builtin_amdgcn_s_memrealtime() - t0 < 2000000ull);
      }
      const bool live = wave + 8 * lane < DROWS && dr0 + wave + 8 * lane < FF;
      const int ha_ = live ? (int)hg[0] : 0, hb_ = live ? (int)hg[2] : 0;
      float v[16];
#pragma unroll
      for (int k = 0; k < 16; ++k) v[k] = 0.f;
#pragma unroll
      for (int k9 = 0; k9 < 9; ++k9) {
        const float ha = __builtin_bit_cast(float, __builtin_amdgcn_readlane(ha_, k9)), hb = __builtin_bit_cast(float, __builtin_amdgcn_readlane(hb_, k9));
#pragma unroll
        for (int k = 0; k < 4; ++k) {
          const float lo = bf_lo(F[k9][k]), hi = bf_hi(F[k9][k]);
          v[2 * k] = fmaf(ha, lo, v[2 * k]); v[2 * k + 1] = fmaf(ha, hi, v[2 * k + 1]);
          v[8 + 2 * k] = fmaf(hb, lo, v[8 + 2 * k]); v[8 + 2 * k + 1] = fmaf(hb, hi, v[8 + 2 * k + 1]);
        }
      }
#pragma unroll
      for (int k = 0; k < 4; ++k)
        *reinterpret_cast<f32x4*>(lds + L_FR + wave * 1024 + (k >> 1) * DM + 8 * lane + 4 * (k & 1)) = f32x4{v[4 * k], v[4 * k + 1], v[4 * k + 2], v[4 * k + 3]};
      if (LAST_ARRIVER()) {
#pragma unroll
        for (int k = 0; k < 8; ++k) {
          f32x2 sacc = {0.f, 0.f};
#pragma unroll
          for (int w = 0; w < NW; ++w) sacc += *reinterpret_cast<const f32x2*>(lds + L_FR + w * 1024 + 2 * (lane + 64 * k));
          st_sc1(wsr, WS_FD + (unsigned)(rid - NG) * (2 * DM * 8) + 16 * (lane + 64 * k), u32x4{__float_as_uint(sacc[0]), T, __float_as_uint(sacc[1]), T});
        }
      }
      SB();
    }
  }
  unsigned* cnt = reinterpret_cast<unsigned*>(lds) + L_CNT;
  unsigned tk = 0;
  if (lane == 0) tk = __hip_atomic_fetch_add(cnt, 2u, __ATOMIC_RELAXED, __HIP_MEMORY_SCOPE_WORKGROUP);
  f32x2 OF[16][4];
#pragma unroll
  for (int t = 0; t < 16; ++t)
#pragma unroll
    for (int k = 0; k < 4; ++k) { OF[t][k] = f32x2{bf_lo(O[t][k]), bf_hi(O[t][k])}; asm volatile("" : "+v"(OF[t][k])); }
  tk = __builtin_amdgcn_readfirstlane(tk);
#pragma unroll 1
  while (tk < tend) {
    unsigned nx = 0;
    if (lane == 0) nx = __hip_atomic_fetch_add(cnt, 2u, __ATOMIC_RELAXED, __HIP_MEMORY_SCOPE_WORKGROUP);
    PROCESS(A); SB(); ISSUE(A, tk); SB();
    PROCESS(B); SB(); ISSUE(B, tk + 1); SB();
    tk = __builtin_amdgcn_readfirstlane(nx);
  }
  PROCESS(A);
  PROCESS(B);
  STAMP_A(5);

  {
    const float mw = xrow_max(m), sc = __builtin_amdgcn_exp2f(m - mw);
    l = xrow_sum(l * sc);
#pragma unroll
    for (int k = 0; k < 8; ++k) acc[k] = xrow_sum(acc[k] * sc);
    float* rec = lds + L_MRG + wave * 132;
    if (r == 0) {
      *reinterpret_cast<f32x4*>(rec + 4 + 8 * c) = f32x4{acc[0], acc[1], acc[2], acc[3]};
      *reinterpret_cast<f32x4*>(rec + 4 + 8 * c + 4) = f32x4{acc[4], acc[5], acc[6], acc[7]};
      if (c == 0) { rec[0] = mw; rec[1] = l; }
    }
  }
  __syncthreads();
  const int e = (b * NH + h) * NSPLIT + split;
  {
    const float* mrg = lds + L_MRG;
    const int sw = lane & 7, g = lane >> 3;
    const float mc = mrg[sw * 132], lc = mrg[sw * 132 + 1];
    float M = mc;
    M = fmaxf(M, dpp<XOR1>(M)); M = fmaxf(M, dpp<XOR2>(M)); M = fmaxf(M, dpp<HMIR>(M));
    const float wgt = __builtin_amdgcn_exp2f(mc - M);
    const f32x2 av = *reinterpret_cast<const f32x2*>(mrg + sw * 132 + 4 + 16 * wave + 2 * g);
    float o0 = av[0] * wgt, o1 = av[1] * wgt, L = lc * wgt;
    o0 += dpp<XOR1>(o0); o0 += dpp<XOR2>(o0); o0 += dpp<HMIR>(o0);
    o1 += dpp<XOR1>(o1); o1 += dpp<XOR2>(o1); o1 += dpp<HMIR>(o1);
    L += dpp<XOR1>(L); L += dpp<XOR2>(L); L += dpp<HMIR>(L);
    f32x2 y[4];
#pragma unroll
    for (int k = 0; k < 4; ++k) y[k] = f32x2{0.f, 0.f};
#pragma unroll
    for (int t = 0; t < 16; ++t) {
      const float ov = __builtin_bit_cast(float, __builtin_amdgcn_readlane(__builtin_bit_cast(int, (t & 1) ? o1 : o0), 8 * (t >> 1)));
#pragma unroll
      for (int k = 0; k < 4; ++k) y[k] = __builtin_elementwise_fma(f32x2{ov, ov}, OF[t][k], y[k]);
    }
    float* yr = lds + L_YR;
    *reinterpret_cast<f32x4*>(yr + wave * DM + 8 * lane) = f32x4{y[0][0], y[0][1], y[1][0], y[1][1]};
    *reinterpret_cast<f32x4*>(yr + wave * DM + 8 * lane + 4) = f32x4{y[2][0], y[2][1], y[3][0], y[3][1]};
    if (tid == 448) st_sc1(wsr, WS_ML + 16 * e, u32x4{__float_as_uint(M), T, __float_as_uint(L), T});
    __syncthreads();
    if (wave < 4) {
      f32x2 s = {0.f, 0.f};
#pragma unroll
      for (int w = 0; w < NW; ++w) s += *reinterpret_cast<const f32x2*>(yr + w * DM + 2 * tid);
      st_sc1(wsr, WS_Y + (unsigned)e * (DM * 8) + 16 * tid, u32x4{__float_as_uint(s[0]), T, __float_as_uint(s[1]), T});
    }
  }
  STAMP_A(6);
  if (ROLE == 3) {
    const unsigned pe = tid >> 1, offy = WS_Y + pe * (DM * 8) + (8 * rid + 4 * (tid & 1)) * 8, offm = WS_ML + 16 * pe;
    u32x4 y0 = ld_sc1(wsr, offy), y1 = ld_sc1(wsr, offy + 16), ml = ld_sc1(wsr, offm);
    if (wave == 7) {
      u32x4 p0 = ld_sc1(wsr, fdoff), p1 = ld_sc1(wsr, fdoff + 8 * (2 * DM * 8)), p2 = ld_sc1(wsr, fdo2);
      bool ok = p0[1] == T && p0[3] == T && p1[1] == T && p1[3] == T && p2[1] == T && p2[3] == T;
      if (!__all(ok)) {
        const unsigned long long t0 = __builtin_amdgcn_s_memrealtime();
        do {
          __builtin_amdgcn_s_sleep(2);
          p0 = ld_sc1(wsr, fdoff); p1 = ld_sc1(wsr, fdoff + 8 * (2 * DM * 8)); p2 = ld_sc1(wsr, fdo2);
          ok = p0[1] == T && p0[3] == T && p1[1] == T && p1[3] == T && p2[1] == T && p2[3] == T;
        } while (!__all(ok) && __builtin_amdgcn_s_memrealtime() - t0 < 2000000ull);
      }
      float f0 = __uint_as_float(p0[0]) + __uint_as_float(p1[0]) + (l2 ? __uint_as_float(p2[0]) : 0.f);
      float f1 = __uint_as_float(p0[2]) + __uint_as_float(p1[2]) + (l2 ? __uint_as_float(p2[2]) : 0.f);
      f0 += dpp<ROR8>(f0); f1 += dpp<ROR8>(f1);
      f0 = xrow_sum(f0); f1 = xrow_sum(f1);
      f0 = rbf(f0); f1 = rbf(f1);
      if (lane < 8) *reinterpret_cast<f32x2*>(lds + L_FIN + 64 + 2 * lane) = f32x2{rbf(f0 / (1.f + __expf(-f0))), rbf(f1 / (1.f + __expf(-f1)))};
    }
    bool ok0 = y0[1] == T && y0[3] == T, ok1 = y1[1] == T && y1[3] == T, okm = ml[1] == T && ml[3] == T;
    if (!__all(ok0 && ok1 && okm)) {
      const unsigned long long t0 = __builtin_amdgcn_s_memrealtime();
      do {
        __builtin_amdgcn_s_sleep(2);
        if (!ok0) { y0 = ld_sc1(wsr, offy); }
        if (!ok1) { y1 = ld_sc1(wsr, offy + 16); }
        if (!okm) { ml = ld_sc1(wsr, offm); }
        ok0 = y0[1] == T && y0[3] == T; ok1 = y1[1] == T && y1[3] == T; okm = ml[1] == T && ml[3] == T;
      } while (!__all(ok0 && ok1 && okm) && __builtin_amdgcn_s_memrealtime() - t0 < 2000000ull);
    }
    const float mm = __uint_as_float(ml[0]), ll = __uint_as_float(ml[2]);
    float M = fmaxf(mm, dpp<XOR2>(mm)); M = fmaxf(M, dpp<HMIR>(M));
    const float ew = __builtin_amdgcn_exp2f(mm - M);
    float L = ew * ll; L += dpp<XOR2>(L); L += dpp<HMIR>(L);
    const float wgt = ew * __builtin_amdgcn_rcpf(L);
    float v0 = __uint_as_float(y0[0]) * wgt, v1 = __uint_as_float(y0[2]) * wgt, v2 = __uint_as_float(y1[0]) * wgt, v3 = __uint_as_float(y1[2]) * wgt;
    v0 = swap32_add(v0, v2); v1 = swap32_add(v1, v3);
    v0 = swap16_add(v0, v1);
    v0 += dpp<ROR8>(v0); v0 += dpp<ROR4>(v0); v0 += dpp<XOR2>(v0);
    if ((lane & 14) == 0) lds[L_FIN + wave * 8 + 4 * (lane & 1) + (lane >> 4)] = v0;
    __syncthreads();
    if (tid < 16) {
      const int bb = tid >> 3, k = tid & 7;
      const float sv = rbf(lds[L_FIN + (4 * bb) * 8 + k] + lds[L_FIN + (4 * bb + 1) * 8 + k] + lds[L_FIN + (4 * bb + 2) * 8 + k] + lds[L_FIN + (4 * bb + 3) * 8 + k]);
      const float g = lds[L_FIN + 64 + tid];
      a.out[bb * DM + 8 * rid + k] = __float2bfloat16(xo + rbf(sv * g));
    }
    if (rid == 0 && tid == 0) __hip_atomic_store(reinterpret_cast<unsigned*>(ws + WS_EPOCH), T, __ATOMIC_RELAXED, __HIP_MEMORY_SCOPE_AGENT);
  }
  STAMP_A(7);
}

__global__ void __launch_bounds__(NTHR) fused_kernel(Args a) {
  __shared__ __attribute__((aligned(16))) float lds[L_END];
  const int xcd = blockIdx.x & 7, j = blockIdx.x >> 3;
  const int g = (j >> 4) * NH + xcd * 4 + (j & 3), split = (j >> 2) & 3;
  if (split == 0) { if (g < NG) body<1>(a, lds, g); else body<2>(a, lds, g); }
  else if (split == 1) body<3>(a, lds, g);
  else body<0>(a, lds, g);
}

extern "C" void kernel_launch(void* const* d_in, const int* in_sizes, int n_in, void* d_out, int out_size, void* d_ws, size_t ws_size,
                              hipStream_t stream) {
  Args a;
  a.x = (const __hip_bfloat16*)d_in[0]; a.Kc = (const __hip_bfloat16*)d_in[1]; a.Vc = (const __hip_bfloat16*)d_in[2];
  a.wq = (const __hip_bfloat16*)d_in[3]; a.wo = (const __hip_bfloat16*)d_in[4]; a.wg = (const __hip_bfloat16*)d_in[6];
  a.wu = (const __hip_bfloat16*)d_in[7]; a.wd = (const __hip_bfloat16*)d_in[8];
  a.out = (__hip_bfloat16*)d_out; a.ws = (unsigned char*)d_ws;
  fused_kernel<<<256, NTHR, 0, stream>>>(a);
}
```

```cpp
#include <hip/hip_runtime.h>
#include <hip/hip_bf16.h>
#include <cstdint>

using u32x4 = __attribute__((ext_vector_type(4))) unsigned;
using f32x4 = __attribute__((ext_vector_type(4))) float;
using f32x2 = __attribute__((ext_vector_type(2))) float;
using bf16x2 = __attribute__((ext_vector_type(2))) __bf16;

constexpr int DM = 512, NH = 32, SK = 8192, DH = 128, FF = 1376;
constexpr int NSPLIT = 4;
constexpr int NW = 8, NTHR = NW * 64, NS = 2;
constexpr int TKEYS = 4 * NS;
constexpr int NT = SK / TKEYS;
constexpr int NPRE = 4;
constexpr int DG = 6, DD = 4;
constexpr int NG = FF / 32, ND = 21, DROWS = 66;
constexpr size_t WS_EPOCH = 0;
constexpr size_t WS_H = 256;
constexpr size_t WS_ML = WS_H + 1376 * 16;
constexpr size_t WS_FD = WS_ML + 256 * 16;
constexpr size_t WS_Y = WS_FD + ND * 2 * DM * 8;
constexpr unsigned long long SPIN_LIMIT = 2000000ull;
#define SB() __builtin_amdgcn_sched_barrier(0)

__device__ __forceinline__ float dot2(unsigned k, unsigned q, float acc) {
  return __builtin_amdgcn_fdot2_f32_bf16(__builtin_bit_cast(bf16x2, k), __builtin_bit_cast(bf16x2, q), acc, false);
}
template <int CTRL> __device__ __forceinline__ float dpp(float x) {
  return __builtin_bit_cast(float, __builtin_amdgcn_mov_dpp(__builtin_bit_cast(int, x), CTRL, 0xf, 0xf, true));
}
constexpr int XOR1 = 0xB1, XOR2 = 0x4E, HMIR = 0x141, ROR8 = 0x128, ROR4 = 0x124;
__device__ __forceinline__ float row16_sum(float x) {
  x += dpp<XOR1>(x); x += dpp<XOR2>(x); x += dpp<HMIR>(x); x += dpp<ROR8>(x); return x;
}
__device__ __forceinline__ float swap16_add(float a, float b) {
  auto s = __builtin_amdgcn_permlane16_swap(__float_as_uint(a), __float_as_uint(b), false, false);
  return __uint_as_float(s[0]) + __uint_as_float(s[1]);
}
__device__ __forceinline__ float swap32_add(float a, float b) {
  auto s = __builtin_amdgcn_permlane32_swap(__float_as_uint(a), __float_as_uint(b), false, false);
  return __uint_as_float(s[0]) + __uint_as_float(s[1]);
}
__device__ __forceinline__ float xrow_sum(float x) { x = swap16_add(x, x); return swap32_add(x, x); }
__device__ __forceinline__ float xrow_max(float x) {
  auto s = __builtin_amdgcn_permlane16_swap(__float_as_uint(x), __float_as_uint(x), false, false);
  x = fmaxf(__uint_as_float(s[0]), __uint_as_float(s[1]));
  auto t = __builtin_amdgcn_permlane32_swap(__float_as_uint(x), __float_as_uint(x), false, false);
  return fmaxf(__uint_as_float(t[0]), __uint_as_float(t[1]));
}
__device__ __forceinline__ float wave_sum(float x) { return xrow_sum(row16_sum(x)); }
__device__ __forceinline__ float bf_lo(unsigned u) { return __uint_as_float(u << 16); }
__device__ __forceinline__ float bf_hi(unsigned u) { return __uint_as_float(u & 0xffff0000u); }
__device__ __forceinline__ float rbf(float x) { return (float)(__bf16)x; }
__device__ __forceinline__ unsigned pk_bf(float lo, float hi) { bf16x2 v = {(__bf16)lo, (__bf16)hi}; return __builtin_bit_cast(unsigned, v); }
__device__ __forceinline__ u32x4 ld_nt(const void* p) { return __builtin_nontemporal_load(reinterpret_cast<const u32x4*>(p)); }
__device__ __forceinline__ u32x4 ld(const void* p) { return *reinterpret_cast<const u32x4*>(p); }
__device__ __forceinline__ void st_sc1(__amdgpu_buffer_rsrc_t rs, unsigned off, u32x4 v) { __builtin_amdgcn_raw_buffer_store_b128(v, rs, off, 0, 16); }
__device__ __forceinline__ u32x4 ld_sc1(__amdgpu_buffer_rsrc_t rs, unsigned off) { return __builtin_amdgcn_raw_buffer_load_b128(rs, off, 0, 16); }

__device__ __forceinline__ void layernorm8(u32x4 xr, float (&xn)[8]) {
  float v[8];
#pragma unroll
  for (int k = 0; k < 4; ++k) { v[2 * k] = bf_lo(xr[k]); v[2 * k + 1] = bf_hi(xr[k]); }
  float s = 0.f;
#pragma unroll
  for (int k = 0; k < 8; ++k) s += v[k];
  const float mean = wave_sum(s) * (1.f / DM);
  float q = 0.f;
#pragma unroll
  for (int k = 0; k < 8; ++k) { const float d = v[k] - mean; q = fmaf(d, d, q); }
  const float var = wave_sum(q) * (1.f / DM);
  const float mb = rbf(mean), den = rbf(__builtin_amdgcn_sqrtf(rbf(rbf(var) + 1e-5f))), rden = __builtin_amdgcn_rcpf(den);
#pragma unroll
  for (int k = 0; k < 8; ++k) xn[k] = rbf(rbf(v[k] - mb) * rden);
}

constexpr int L_YR = 0;
constexpr int L_XP = 0;
constexpr int L_QP = L_XP + 8 * 256;
constexpr int L_MRG = NW * 512;
constexpr int L_XS = L_MRG + NW * 132;
constexpr int L_FIN = L_XS + 1024;
constexpr int L_CNT = L_FIN + 64 + 16;
constexpr int L_FR = L_CNT + 4;
constexpr int L_END = L_FR + NW * 1024;

struct Args {
  const __hip_bfloat16 *x, *Kc, *Vc, *wq, *wo, *wg, *wu, *wd;
  __hip_bfloat16* out;
  unsigned char* ws;
};

template <int ROLE, bool LATEQ>
__device__ __forceinline__ void body(const Args& a, float* lds, const int rid  ) {
  unsigned char* ws = a.ws;
  constexpr int T_STAT = LATEQ ? 8 * NPRE : 4 * NPRE;
  constexpr int NSTATIC = T_STAT + 18 * NW;
  const int tid = threadIdx.x, lane = tid & 63, wave = __builtin_amdgcn_readfirstlane(tid >> 6), r = lane >> 4, c = lane & 15;
  const int bid = blockIdx.x, xcd = bid & 7, j = bid >> 3;
  const int h = xcd * 4 + (j & 3), b = j >> 4, split = (j >> 2) & 3;
  const int rot = 128 * (2 * b + (split >> 1));
  const __amdgpu_buffer_rsrc_t wsr = __builtin_amdgcn_make_buffer_rsrc(ws, 0, 4u << 20, 0x00020000);

  const size_t kvbase = ((size_t)(b * NH + h) * SK + r) * DH + 8 * c;
  const int dd = rid < NG ? DG : DD;
  const int ts = split == 0 ? 0 : 256 - 3 * dd + (split - 1) * (256 + dd);
  const unsigned tend = split == 0 ? 256 - 3 * dd : ts + 256 + dd;
  const __hip_bfloat16* kp = a.Kc + kvbase;
  const __hip_bfloat16* vp = a.Vc + kvbase;
#define ISSUE(BUF, T) do { const size_t o_ = (size_t)(T) * (TKEYS * DH); \
    _Pragma("unroll") for (int t_ = 0; t_ < NS; ++t_) (BUF)[t_] = ld_nt(kp + o_ + 4 * t_ * DH); \
    _Pragma("unroll") for (int t_ = 0; t_ < NS; ++t_) (BUF)[NS + t_] = ld_nt(vp + o_ + 4 * t_ * DH); } while (0)

  u32x4 E[32];
  unsigned epoch = 0;
  if (LATEQ) {
#pragma unroll
    for (int i = 0; i < NPRE; ++i) ISSUE(E + 4 * i, ts + 8 * i + wave);
    SB();
    const u32x4 x0 = ld(a.x + b * DM + 8 * lane);
    epoch = __hip_atomic_load(reinterpret_cast<const unsigned*>(ws + WS_EPOCH), __ATOMIC_RELAXED, __HIP_MEMORY_SCOPE_AGENT);
    if (tid == 0) { reinterpret_cast<unsigned*>(lds)[L_CNT] = ts + NSTATIC; reinterpret_cast<unsigned*>(lds)[L_CNT + 1] = 0; }
    SB();
    {
      const __hip_bfloat16* p0 = a.wq + (size_t)(64 * wave + r) * (NH * DH) + h * DH + 8 * c;
#pragma unroll
      for (int i = 0; i < 16; ++i) E[16 + i] = ld_nt(p0 + (size_t)(4 * i) * (NH * DH));
    }
    SB();
    {
      float n0[8];
      layernorm8(x0, n0);
      u32x4 pk;
#pragma unroll
      for (int k = 0; k < 4; ++k) pk[k] = pk_bf(n0[k], n0[k + 4]);
      *reinterpret_cast<u32x4*>(lds + L_XP + wave * 256 + 4 * lane) = pk;
    }
    float qa[8];
#pragma unroll
    for (int k = 0; k < 8; ++k) qa[k] = 0.f;
    const unsigned* xp = reinterpret_cast<const unsigned*>(lds + L_XP + wave * 256 + 32 * wave + r);
#pragma unroll
    for (int p = 0; p < 8; ++p) {
      const unsigned xv = xp[4 * p];
#pragma unroll
      for (int k = 0; k < 4; ++k) {
        const unsigned lo = __builtin_amdgcn_perm(E[16 + 2 * p + 1][k], E[16 + 2 * p][k], 0x05040100u);
        const unsigned hi = __builtin_amdgcn_perm(E[16 + 2 * p + 1][k], E[16 + 2 * p][k], 0x07060302u);
        qa[2 * k] = dot2(lo, xv, qa[2 * k]); qa[2 * k + 1] = dot2(hi, xv, qa[2 * k + 1]);
      }
    }
#pragma unroll
    for (int k = 0; k < 8; ++k) qa[k] = xrow_sum(qa[k]);
    if (r == 0) {
      *reinterpret_cast<f32x4*>(lds + L_QP + wave * DH + 8 * c) = f32x4{qa[0], qa[1], qa[2], qa[3]};
      *reinterpret_cast<f32x4*>(lds + L_QP + wave * DH + 8 * c + 4) = f32x4{qa[4], qa[5], qa[6], qa[7]};
    }
  } else if (wave < 4) {
    u32x4 xr;
    if (ROLE == 1) xr = ld(a.x + (wave & 1) * DM + 8 * lane);
    epoch = __hip_atomic_load(reinterpret_cast<const unsigned*>(ws + WS_EPOCH), __ATOMIC_RELAXED, __HIP_MEMORY_SCOPE_AGENT);
    if (tid == 0) { reinterpret_cast<unsigned*>(lds)[L_CNT] = ts + NSTATIC; reinterpret_cast<unsigned*>(lds)[L_CNT + 1] = 0; }
    SB();
    __builtin_amdgcn_s_barrier();
    SB();
#pragma unroll
    for (int i = 0; i < NPRE; ++i) ISSUE(E + 4 * i, ts + 4 * i + wave);
    SB();
    if (ROLE == 1) {
      float n0[8];
      layernorm8(xr, n0);
      if (wave < 2) {
        *reinterpret_cast<f32x4*>(lds + L_XS + wave * DM + 8 * lane) = f32x4{n0[0], n0[1], n0[2], n0[3]};
        *reinterpret_cast<f32x4*>(lds + L_XS + wave * DM + 8 * lane + 4) = f32x4{n0[4], n0[5], n0[6], n0[7]};
      }
    }
  } else {
    const int qw = wave - 4;
    const int rb0 = (128 * qw + rot) & 511, rb1 = (128 * qw + 64 + rot) & 511;
    const u32x4 x0 = ld(a.x + b * DM + 8 * lane);
    SB();
    {
      const __hip_bfloat16* p0 = a.wq + (size_t)(rb0 + r) * (NH * DH) + h * DH + 8 * c;
      const __hip_bfloat16* p1 = a.wq + (size_t)(rb1 + r) * (NH * DH) + h * DH + 8 * c;
#pragma unroll
      for (int i = 0; i < 16; ++i) E[i] = ld(p0 + (size_t)(4 * i) * (NH * DH));
#pragma unroll
      for (int i = 0; i < 16; ++i) E[16 + i] = ld(p1 + (size_t)(4 * i) * (NH * DH));
    }
    epoch = __hip_atomic_load(reinterpret_cast<const unsigned*>(ws + WS_EPOCH), __ATOMIC_RELAXED, __HIP_MEMORY_SCOPE_AGENT);
    SB();
    __builtin_amdgcn_s_barrier();
    SB();
    {
      float n0[8];
      layernorm8(x0, n0);
      u32x4 pk;
#pragma unroll
      for (int k = 0; k < 4; ++k) pk[k] = pk_bf(n0[k], n0[k + 4]);
      *reinterpret_cast<u32x4*>(lds + L_XP + qw * 256 + 4 * lane) = pk;
    }
    float qa[8];
#pragma unroll
    for (int k = 0; k < 8; ++k) qa[k] = 0.f;
    const unsigned* xp0 = reinterpret_cast<const unsigned*>(lds + L_XP + qw * 256 + rb0 / 2 + r);
    const unsigned* xp1 = reinterpret_cast<const unsigned*>(lds + L_XP + qw * 256 + rb1 / 2 + r);
#pragma unroll
    for (int p = 0; p < 16; ++p) {
      const unsigned xv = p < 8 ? xp0[4 * p] : xp1[4 * (p - 8)];
#pragma unroll
      for (int k = 0; k < 4; ++k) {
        const unsigned lo = __builtin_amdgcn_perm(E[2 * p + 1][k], E[2 * p][k], 0x05040100u);
        const unsigned hi = __builtin_amdgcn_perm(E[2 * p + 1][k], E[2 * p][k], 0x07060302u);
        qa[2 * k] = dot2(lo, xv, qa[2 * k]); qa[2 * k + 1] = dot2(hi, xv, qa[2 * k + 1]);
      }
    }
#pragma unroll
    for (int k = 0; k < 8; ++k) qa[k] = xrow_sum(qa[k]);
    if (r == 0) {
      *reinterpret_cast<f32x4*>(lds + L_QP + qw * DH + 8 * c) = f32x4{qa[0], qa[1], qa[2], qa[3]};
      *reinterpret_cast<f32x4*>(lds + L_QP + qw * DH + 8 * c + 4) = f32x4{qa[4], qa[5], qa[6], qa[7]};
    }
  }
  __syncthreads();
  u32x4 A[2 * NS], B[2 * NS];
  ISSUE(A, ts + T_STAT + wave);
  ISSUE(B, ts + T_STAT + NW + wave);
  SB();
  u32x4 q;
  {
    f32x4 s0 = *reinterpret_cast<const f32x4*>(lds + L_QP + r * DH + 8 * c);
    f32x4 s1 = *reinterpret_cast<const f32x4*>(lds + L_QP + r * DH + 8 * c + 4);
    if (LATEQ) { s0 += *reinterpret_cast<const f32x4*>(lds + L_QP + (4 + r) * DH + 8 * c); s1 += *reinterpret_cast<const f32x4*>(lds + L_QP + (4 + r) * DH + 8 * c + 4); }
    float qa[8] = {s0[0], s0[1], s0[2], s0[3], s1[0], s1[1], s1[2], s1[3]};
#pragma unroll
    for (int k = 0; k < 8; ++k) qa[k] = xrow_sum(qa[k]);
#pragma unroll
    for (int k = 0; k < 4; ++k) q[k] = pk_bf(qa[2 * k], qa[2 * k + 1]);
  }
  const unsigned T = __builtin_amdgcn_readfirstlane(epoch) + 1u;

  constexpr float SCALE = 0.08838834764831845f, LOG2E = 1.4426950408889634f;
  float m = -INFINITY, l = 0.f, acc[8];
#pragma unroll
  for (int k = 0; k < 8; ++k) acc[k] = 0.f;
#define PROCESS(BUF) do { float s_[NS]; float mn_ = m; \
    _Pragma("unroll") for (int t_ = 0; t_ < NS; ++t_) { float d_ = dot2((BUF)[t_][0], q[0], 0.f); d_ = dot2((BUF)[t_][1], q[1], d_); d_ = dot2((BUF)[t_][2], q[2], d_); d_ = dot2((BUF)[t_][3], q[3], d_); \
      d_ = row16_sum(d_); s_[t_] = rbf(rbf(d_) * SCALE) * LOG2E; mn_ = fmaxf(mn_, s_[t_]); } \
    const float al_ = __builtin_amdgcn_exp2f(m - mn_); m = mn_; l *= al_; \
    _Pragma("unroll") for (int k_ = 0; k_ < 8; ++k_) acc[k_] *= al_; \
    _Pragma("unroll") for (int t_ = 0; t_ < NS; ++t_) { const float p_ = __builtin_amdgcn_exp2f(s_[t_] - mn_); l += p_; \
      _Pragma("unroll") for (int k_ = 0; k_ < 4; ++k_) { acc[2 * k_] = fmaf(p_, bf_lo((BUF)[NS + t_][k_]), acc[2 * k_]); acc[2 * k_ + 1] = fmaf(p_, bf_hi((BUF)[NS + t_][k_]), acc[2 * k_ + 1]); } } \
    asm volatile("" : "+v"(acc[0]), "+v"(acc[1]), "+v"(acc[2]), "+v"(acc[3]), "+v"(acc[4]), "+v"(acc[5]), "+v"(acc[6]), "+v"(acc[7]), "+v"(m), "+v"(l)); } while (0)

  if (LATEQ || wave < 4) {
#pragma unroll
    for (int i = 0; i < NPRE; ++i) { PROCESS(E + 4 * i); SB(); }
  }

  u32x4 O[16];
  u32x4 F[9], hg;
  float xo = 0.f;
  const __hip_bfloat16* wop = a.wo + (size_t)(h * DH + 16 * wave) * DM + 8 * lane;
  const int dr0 = DROWS * (rid - NG);
  const unsigned hoff = WS_H + 16 * (unsigned)min(dr0 + wave + 8 * min(lane, 8), FF - 1);
  const unsigned fdoff = WS_FD + (unsigned)(((lane >> 3) * 2 + ((lane >> 2) & 1)) * DM + 8 * rid + 2 * (lane & 3)) * 8;
  const bool l2 = (lane >> 3) + 16 < ND;
  const unsigned fdo2 = l2 ? fdoff + 16 * (2 * DM * 8) : fdoff;
#define LAST_ARRIVER() ({ unsigned arr_ = 0; asm volatile("s_waitcnt lgkmcnt(0)" ::: "memory"); \
      if (lane == 0) arr_ = __hip_atomic_fetch_add(reinterpret_cast<unsigned*>(lds) + L_CNT + 1, 1u, __ATOMIC_RELAXED, __HIP_MEMORY_SCOPE_WORKGROUP); \
      arr_ = __builtin_amdgcn_readfirstlane(arr_); asm volatile("" ::: "memory"); arr_ == NW - 1; })
#pragma unroll
  for (int i = 0; i < 16; ++i) {
    u32x4* buf = (i & 1) ? B : A;
    PROCESS(buf); SB(); O[i] = ld(wop + (size_t)i * DM); SB(); ISSUE(buf, ts + T_STAT + NW * (i + 2) + wave);
    if (ROLE == 1 && i < 4) {
      const size_t o = (size_t)(64 * wave + 16 * i + (lane >> 2)) * FF + 32 * rid + 8 * (lane & 3);
      F[i] = ld(a.wg + o); F[4 + i] = ld(a.wu + o);
    }
    if (ROLE == 2 && i >= 2 && i < 7) {
#pragma unroll
      for (int k = 2 * (i - 2); k < 2 * (i - 2) + 2; ++k) if (k < 9) F[k] = ld_nt(a.wd + (size_t)min(dr0 + wave + 8 * k, FF - 1) * DM + 8 * lane);
    }
    if (ROLE == 2 && i == 12) hg = ld_sc1(wsr, hoff);
    if (ROLE == 3 && i == 13) {
      xo = __bfloat162float(a.x[((tid >> 3) & 1) * DM + 8 * rid + (tid & 7)]);
    }
    SB();
    if (ROLE == 1 && i == 5) {
      float v[32];
#pragma unroll
      for (int k = 0; k < 32; ++k) v[k] = 0.f;
#pragma unroll
      for (int ii = 0; ii < 4; ++ii) {
        const float xa = lds[L_XS + 64 * wave + 16 * ii + (lane >> 2)], xb = lds[L_XS + DM + 64 * wave + 16 * ii + (lane >> 2)];
#pragma unroll
        for (int k = 0; k < 4; ++k) {
          const float gl = bf_lo(F[ii][k]), gh = bf_hi(F[ii][k]), ul = bf_lo(F[4 + ii][k]), uh = bf_hi(F[4 + ii][k]);
          v[2 * k] = fmaf(xa, gl, v[2 * k]); v[2 * k + 1] = fmaf(xa, gh, v[2 * k + 1]);
          v[8 + 2 * k] = fmaf(xb, gl, v[8 + 2 * k]); v[8 + 2 * k + 1] = fmaf(xb, gh, v[8 + 2 * k + 1]);
          v[16 + 2 * k] = fmaf(xa, ul, v[16 + 2 * k]); v[16 + 2 * k + 1] = fmaf(xa, uh, v[16 + 2 * k + 1]);
          v[24 + 2 * k] = fmaf(xb, ul, v[24 + 2 * k]); v[24 + 2 * k + 1] = fmaf(xb, uh, v[24 + 2 * k + 1]);
        }
      }
#pragma unroll
      for (int k = 0; k < 16; ++k) v[k] = swap32_add(v[k], v[k + 16]);
#pragma unroll
      for (int k = 0; k < 8; ++k) v[k] = swap16_add(v[k], v[k + 8]);
#pragma unroll
      for (int k = 0; k < 4; ++k) { const float t1 = v[k] + dpp<ROR8>(v[k]), t2 = v[k + 4] + dpp<ROR8>(v[k + 4]); v[k] = (lane & 8) ? t2 : t1; }
      *reinterpret_cast<f32x4*>(lds + L_FR + wave * 256 + ((lane >> 2) & 1) * 128 + (lane >> 4) * 32 + 8 * (lane & 3) + 4 * ((lane >> 3) & 1)) = f32x4{v[0], v[1], v[2], v[3]};
      if (LAST_ARRIVER() && lane < 32) {
        float hv[2];
#pragma unroll
        for (int bb = 0; bb < 2; ++bb) {
          float g = 0.f, u = 0.f;
#pragma unroll
          for (int w = 0; w < 2 * NW; ++w) { g += lds[L_FR + w * 128 + bb * 32 + lane]; u += lds[L_FR + w * 128 + 64 + bb * 32 + lane]; }
          g = rbf(g); u = rbf(u);
          const float ge = rbf(0.5f * g * (1.f + tanhf(0.7978845608028654f * (g + 0.044715f * g * g * g))));
          hv[bb] = rbf(ge * u);
        }
        st_sc1(wsr, WS_H + 16 * (32 * rid + lane), u32x4{__float_as_uint(hv[0]), T, __float_as_uint(hv[1]), T});
      }
      SB();
    }
    if (ROLE == 2 && i == 14) {
      if (!__all(hg[1] == T && hg[3] == T)) {
        const unsigned long long t0 = __builtin_amdgcn_s_memrealtime();
        do { __builtin_amdgcn_s_sleep(8); hg = ld_sc1(wsr, hoff); } while (!__all(hg[1] == T && hg[3] == T) && __builtin_amdgcn_s_memrealtime() - t0 < SPIN_LIMIT);
      }
      const bool live = wave + 8 * lane < DROWS && dr0 + wave + 8 * lane < FF;
      const int ha_ = live ? (int)hg[0] : 0, hb_ = live ? (int)hg[2] : 0;
      float v[16];
#pragma unroll
      for (int k = 0; k < 16; ++k) v[k] = 0.f;
#pragma unroll
      for (int k9 = 0; k9 < 9; ++k9) {
        const float ha = __builtin_bit_cast(float, __builtin_amdgcn_readlane(ha_, k9)), hb = __builtin_bit_cast(float, __builtin_amdgcn_readlane(hb_, k9));
#pragma unroll
        for (int k = 0; k < 4; ++k) {
          const float lo = bf_lo(F[k9][k]), hi = bf_hi(F[k9][k]);
          v[2 * k] = fmaf(ha, lo, v[2 * k]); v[2 * k + 1] = fmaf(ha, hi, v[2 * k + 1]);
          v[8 + 2 * k] = fmaf(hb, lo, v[8 + 2 * k]); v[8 + 2 * k + 1] = fmaf(hb, hi, v[8 + 2 * k + 1]);
        }
      }
#pragma unroll
      for (int k = 0; k < 4; ++k)
        *reinterpret_cast<f32x4*>(lds + L_FR + wave * 1024 + (k >> 1) * DM + 8 * lane + 4 * (k & 1)) = f32x4{v[4 * k], v[4 * k + 1], v[4 * k + 2], v[4 * k + 3]};
      if (LAST_ARRIVER()) {
#pragma unroll
        for (int k = 0; k < 8; ++k) {
          f32x2 sacc = {0.f, 0.f};
#pragma unroll
          for (int w = 0; w < NW; ++w) sacc += *reinterpret_cast<const f32x2*>(lds + L_FR + w * 1024 + 2 * (lane + 64 * k));
          st_sc1(wsr, WS_FD + (unsigned)(rid - NG) * (2 * DM * 8) + 16 * (lane + 64 * k), u32x4{__float_as_uint(sacc[0]), T, __float_as_uint(sacc[1]), T});
        }
      }
      SB();
    }
  }
  unsigned* cnt = reinterpret_cast<unsigned*>(lds) + L_CNT;
  unsigned tk = 0;
  if (lane == 0) tk = __hip_atomic_fetch_add(cnt, 2u, __ATOMIC_RELAXED, __HIP_MEMORY_SCOPE_WORKGROUP);
  f32x2 OF[16][4];
#pragma unroll
  for (int t = 0; t < 16; ++t)
#pragma unroll
    for (int k = 0; k < 4; ++k) { OF[t][k] = f32x2{bf_lo(O[t][k]), bf_hi(O[t][k])}; asm volatile("" : "+v"(OF[t][k])); }
  tk = __builtin_amdgcn_readfirstlane(tk);
#pragma unroll 1
  while (tk < tend) {
    unsigned nx = 0;
    if (lane == 0) nx = __hip_atomic_fetch_add(cnt, 2u, __ATOMIC_RELAXED, __HIP_MEMORY_SCOPE_WORKGROUP);
    PROCESS(A); SB(); ISSUE(A, tk); SB();
    PROCESS(B); SB(); ISSUE(B, tk + 1); SB();
    tk = __builtin_amdgcn_readfirstlane(nx);
  }
  PROCESS(A);
  PROCESS(B);

  {
    const float mw = xrow_max(m), sc = __builtin_amdgcn_exp2f(m - mw);
    l = xrow_sum(l * sc);
#pragma unroll
    for (int k = 0; k < 8; ++k) acc[k] = xrow_sum(acc[k] * sc);
    float* rec = lds + L_MRG + wave * 132;
    if (r == 0) {
      *reinterpret_cast<f32x4*>(rec + 4 + 8 * c) = f32x4{acc[0], acc[1], acc[2], acc[3]};
      *reinterpret_cast<f32x4*>(rec + 4 + 8 * c + 4) = f32x4{acc[4], acc[5], acc[6], acc[7]};
      if (c == 0) { rec[0] = mw; rec[1] = l; }
    }
  }
  __syncthreads();
  const int e = (b * NH + h) * NSPLIT + split;
  {
    const float* mrg = lds + L_MRG;
    const int sw = lane & 7, g = lane >> 3;
    const float mc = mrg[sw * 132], lc = mrg[sw * 132 + 1];
    float M = mc;
    M = fmaxf(M, dpp<XOR1>(M)); M = fmaxf(M, dpp<XOR2>(M)); M = fmaxf(M, dpp<HMIR>(M));
    const float wgt = __builtin_amdgcn_exp2f(mc - M);
    const f32x2 av = *reinterpret_cast<const f32x2*>(mrg + sw * 132 + 4 + 16 * wave + 2 * g);
    float o0 = av[0] * wgt, o1 = av[1] * wgt, L = lc * wgt;
    o0 += dpp<XOR1>(o0); o0 += dpp<XOR2>(o0); o0 += dpp<HMIR>(o0);
    o1 += dpp<XOR1>(o1); o1 += dpp<XOR2>(o1); o1 += dpp<HMIR>(o1);
    L += dpp<XOR1>(L); L += dpp<XOR2>(L); L += dpp<HMIR>(L);
    f32x2 y[4];
#pragma unroll
    for (int k = 0; k < 4; ++k) y[k] = f32x2{0.f, 0.f};
#pragma unroll
    for (int t = 0; t < 16; ++t) {
      const float ov = __builtin_bit_cast(float, __builtin_amdgcn_readlane(__builtin_bit_cast(int, (t & 1) ? o1 : o0), 8 * (t >> 1)));
#pragma unroll
      for (int k = 0; k < 4; ++k) y[k] = __builtin_elementwise_fma(f32x2{ov, ov}, OF[t][k], y[k]);
    }
    float* yr = lds + L_YR;
    *reinterpret_cast<f32x4*>(yr + wave * DM + 8 * lane) = f32x4{y[0][0], y[0][1], y[1][0], y[1][1]};
    *reinterpret_cast<f32x4*>(yr + wave * DM + 8 * lane + 4) = f32x4{y[2][0], y[2][1], y[3][0], y[3][1]};
    if (tid == 448) st_sc1(wsr, WS_ML + 16 * e, u32x4{__float_as_uint(M), T, __float_as_uint(L), T});
    __syncthreads();
    if (wave < 4) {
      f32x2 s = {0.f, 0.f};
#pragma unroll
      for (int w = 0; w < NW; ++w) s += *reinterpret_cast<const f32x2*>(yr + w * DM + 2 * tid);
      st_sc1(wsr, WS_Y + (unsigned)e * (DM * 8) + 16 * tid, u32x4{__float_as_uint(s[0]), T, __float_as_uint(s[1]), T});
    }
  }
  if (ROLE == 3) {
    const unsigned pe = tid >> 1, offy = WS_Y + pe * (DM * 8) + (8 * rid + 4 * (tid & 1)) * 8, offm = WS_ML + 16 * pe;
    u32x4 y0 = ld_sc1(wsr, offy), y1 = ld_sc1(wsr, offy + 16), ml = ld_sc1(wsr, offm);
    if (wave == 7) {
      u32x4 p0 = ld_sc1(wsr, fdoff), p1 = ld_sc1(wsr, fdoff + 8 * (2 * DM * 8)), p2 = ld_sc1(wsr, fdo2);
      bool ok = p0[1] == T && p0[3] == T && p1[1] == T && p1[3] == T && p2[1] == T && p2[3] == T;
      if (!__all(ok)) {
        const unsigned long long t0 = __builtin_amdgcn_s_memrealtime();
        do {
          __builtin_amdgcn_s_sleep(2);
          p0 = ld_sc1(wsr, fdoff); p1 = ld_sc1(wsr, fdoff + 8 * (2 * DM * 8)); p2 = ld_sc1(wsr, fdo2);
          ok = p0[1] == T && p0[3] == T && p1[1] == T && p1[3] == T && p2[1] == T && p2[3] == T;
        } while (!__all(ok) && __builtin_amdgcn_s_memrealtime() - t0 < SPIN_LIMIT);
      }
      float f0 = __uint_as_float(p0[0]) + __uint_as_float(p1[0]) + (l2 ? __uint_as_float(p2[0]) : 0.f);
      float f1 = __uint_as_float(p0[2]) + __uint_as_float(p1[2]) + (l2 ? __uint_as_float(p2[2]) : 0.f);
      f0 += dpp<ROR8>(f0); f1 += dpp<ROR8>(f1);
      f0 = xrow_sum(f0); f1 = xrow_sum(f1);
      f0 = rbf(f0); f1 = rbf(f1);
      if (lane < 8) *reinterpret_cast<f32x2*>(lds + L_FIN + 64 + 2 * lane) = f32x2{rbf(f0 / (1.f + __expf(-f0))), rbf(f1 / (1.f + __expf(-f1)))};
    }
    bool ok0 = y0[1] == T && y0[3] == T, ok1 = y1[1] == T && y1[3] == T, okm = ml[1] == T && ml[3] == T;
    if (!__all(ok0 && ok1 && okm)) {
      const unsigned long long t0 = __builtin_amdgcn_s_memrealtime();
      do {
        __builtin_amdgcn_s_sleep(2);
        if (!ok0) { y0 = ld_sc1(wsr, offy); }
        if (!ok1) { y1 = ld_sc1(wsr, offy + 16); }
        if (!okm) { ml = ld_sc1(wsr, offm); }
        ok0 = y0[1] == T && y0[3] == T; ok1 = y1[1] == T && y1[3] == T; okm = ml[1] == T && ml[3] == T;
      } while (!__all(ok0 && ok1 && okm) && __builtin_amdgcn_s_memrealtime() - t0 < SPIN_LIMIT);
    }
    const float mm = __uint_as_float(ml[0]), ll = __uint_as_float(ml[2]);
    float M = fmaxf(mm, dpp<XOR2>(mm)); M = fmaxf(M, dpp<HMIR>(M));
    const float ew = __builtin_amdgcn_exp2f(mm - M);
    float L = ew * ll; L += dpp<XOR2>(L); L += dpp<HMIR>(L);
    const float wgt = ew * __builtin_amdgcn_rcpf(L);
    float v0 = __uint_as_float(y0[0]) * wgt, v1 = __uint_as_float(y0[2]) * wgt, v2 = __uint_as_float(y1[0]) * wgt, v3 = __uint_as_float(y1[2]) * wgt;
    v0 = swap32_add(v0, v2); v1 = swap32_add(v1, v3);
    v0 = swap16_add(v0, v1);
    v0 += dpp<ROR8>(v0); v0 += dpp<ROR4>(v0); v0 += dpp<XOR2>(v0);
    if ((lane & 14) == 0) lds[L_FIN + wave * 8 + 4 * (lane & 1) + (lane >> 4)] = v0;
    __syncthreads();
    if (tid < 16) {
      const int bb = tid >> 3, k = tid & 7;
      const float sv = rbf(lds[L_FIN + (4 * bb) * 8 + k] + lds[L_FIN + (4 * bb + 1) * 8 + k] + lds[L_FIN + (4 * bb + 2) * 8 + k] + lds[L_FIN + (4 * bb + 3) * 8 + k]);
      const float g = lds[L_FIN + 64 + tid];
      a.out[bb * DM + 8 * rid + k] = __float2bfloat16(xo + rbf(sv * g));
    }
    if (rid == 0 && tid == 0) __hip_atomic_store(reinterpret_cast<unsigned*>(ws + WS_EPOCH), T, __ATOMIC_RELAXED, __HIP_MEMORY_SCOPE_AGENT);
  }
}

__global__ void __launch_bounds__(NTHR, 2) fused_kernel(Args a) {
  __shared__ __attribute__((aligned(16))) float lds[L_END];
  const int xcd = blockIdx.x & 7, j = blockIdx.x >> 3;
  const int g = (j >> 4) * NH + xcd * 4 + (j & 3), split = (j >> 2) & 3;
  if (split == 0) { if (g < NG) body<1, false>(a, lds, g); else body<2, false>(a, lds, g); }
  else if (split == 1) body<3, true>(a, lds, g);
  else if (split == 2) body<0, false>(a, lds, g);
  else body<0, true>(a, lds, g);
}

extern "C" void kernel_launch(void* const* d_in, const int* in_sizes, int n_in, void* d_out, int out_size, void* d_ws, size_t ws_size,
                              hipStream_t stream) {
  Args a;
  a.x = (const __hip_bfloat16*)d_in[0]; a.Kc = (const __hip_bfloat16*)d_in[1]; a.Vc = (const __hip_bfloat16*)d_in[2];
  a.wq = (const __hip_bfloat16*)d_in[3]; a.wo = (const __hip_bfloat16*)d_in[4]; a.wg = (const __hip_bfloat16*)d_in[6];
  a.wu = (const __hip_bfloat16*)d_in[7]; a.wd = (const __hip_bfloat16*)d_in[8];
  a.out = (__hip_bfloat16*)d_out; a.ws = (unsigned char*)d_ws;
  fused_kernel<<<256, NTHR, 0, stream>>>(a);
}
```
